```python
import torch

def _softmax(x, axis=-1):
    return torch.softmax(x, dim=axis)
def _gelu(x):
    return torch.nn.functional.gelu(x, approximate="tanh")
def _silu(x):
    return torch.nn.functional.silu(x)
def _sigmoid(x):
    return torch.sigmoid(x)
def _layernorm(x, axis=-1, eps=1e-5):
    m = x.mean(dim=axis, keepdim=True)
    v = x.var(dim=axis, keepdim=True, unbiased=False)
    return (x - m) / torch.sqrt(v + eps)
def _scatter_add(tgt, idx, src, axis=0):
    return tgt.clone().index_add_(axis, idx, src)
def _segment_sum(src, seg, n):
    out = torch.zeros((n,) + src.shape[1:], dtype=src.dtype)
    return out.index_add_(0, seg, src)
def _pad(x, pw):
    flat = tuple(v for pair in reversed(pw) for v in pair)
    return torch.nn.functional.pad(x, flat)

kernel_name = 'sequential_preLN__linear_attn_block__dense_activation_mish__prompt'

def setup_inputs():

    _w = (lambda a: (lambda t: t.to(torch.bfloat16) if t.is_floating_point() else t)(torch.as_tensor(a)))
    def _A_setup():
        B, S, D = 1, 2048, 512
        return {
            "x": _w((lambda *s: torch.randn(*s, dtype=torch.bfloat16) * 0.05)(B, S, D)),
            "wqkv": _w((lambda *s: torch.randn(*s, dtype=torch.bfloat16) * 0.05)(D, 3 * D)),
            "wo": _w((lambda *s: torch.randn(*s, dtype=torch.bfloat16) * 0.05)(D, D)),
        }
    _A_in = _A_setup()

    def _B_setup():
        B, S, D, F = 1, 2048, 512, 1376
        return {
            "x": _w((lambda *s: torch.randn(*s, dtype=torch.bfloat16) * 0.05)(B, S, D)),
            "w1": _w((lambda *s: torch.randn(*s, dtype=torch.bfloat16) * 0.8)(D, F) * (D ** -0.5)),
            "b1": _w((lambda *s: torch.randn(*s, dtype=torch.bfloat16) * 0.2)(F)),
            "w2": _w((lambda *s: torch.randn(*s, dtype=torch.bfloat16) * 0.1)(F, D) * (F ** -0.5)),
        }
    _B_in = _B_setup()

    return {
        "A_x": _A_in["x"],
        "A_wqkv": _A_in["wqkv"],
        "A_wo": _A_in["wo"],
        "B_x": _B_in["x"],
        "B_w1": _B_in["w1"],
        "B_b1": _B_in["b1"],
        "B_w2": _B_in["w2"],
    }

def reference(A_x, A_wqkv, A_wo, B_x, B_w1, B_b1, B_w2):
    def _A_ref(x, wqkv, wo):
        B, S, D = x.shape
        H = 32; Dh = D // H
        qkv = (lambda x, *d: x.permute(*d))((x @ wqkv).reshape(B, S, 3, H, Dh), 2, 0, 3, 1, 4)
        q, k, v = qkv[0], qkv[1], qkv[2]
        phi = lambda t: torch.where(t > 0, t + 1, torch.exp(t)) + 1e-6
        pq, pk = phi(q), phi(k)
        kv = torch.einsum("bhsd,bhse->bhde", pk, v)
        z = torch.einsum("bhsd->bhd", pk)
        num = torch.einsum("bhqd,bhde->bhqe", pq, kv)
        den = torch.einsum("bhqd,bhd->bhq", pq, z)[..., None] + 1e-6
        o = num / den
        return (lambda x, *d: x.permute(*d))(o, 0, 2, 1, 3).reshape(B, S, D) @ wo

    def _B_ref(x, w1, b1, w2):
        h = torch.einsum("bsd,df->bsf", x, w1) + b1
        sp = torch.log1p(torch.exp(-torch.abs(h))) + torch.where(h > 0, h, torch.zeros_like(h))
        act = h * torch.tanh(sp)
        return torch.einsum("bsf,fd->bsd", act, w2)

    x = A_x
    h = x + _A_ref(_layernorm(x, axis=-1), A_wqkv, A_wo)
    h2 = h + _B_ref(_layernorm(h, axis=-1), B_w1, B_b1, B_w2)
    return h2
```

```cpp
#include <hip/hip_runtime.h>
#include <hip/hip_bf16.h>
#include <stdint.h>

#if defined(__HIP_DEVICE_COMPILE__)
#define NOPK __attribute__((target("no-packed-fp32-ops")))
#else
#define NOPK
#endif
#define DI __device__ __forceinline__ NOPK
#define SB() __builtin_amdgcn_sched_barrier(0)
#define LAS __attribute__((address_space(3)))
#define GAS __attribute__((address_space(1)))
typedef unsigned v4u __attribute__((ext_vector_type(4)));
typedef unsigned v2u __attribute__((ext_vector_type(2)));
typedef short s16x4 __attribute__((ext_vector_type(4)));
typedef __bf16 bf16x8 __attribute__((ext_vector_type(8)));
typedef __bf16 bf16x2 __attribute__((ext_vector_type(2)));
typedef float f32x16 __attribute__((ext_vector_type(16)));
typedef float f32x4 __attribute__((ext_vector_type(4)));
typedef float f32x2 __attribute__((ext_vector_type(2)));
typedef unsigned long long u64;
typedef __amdgpu_buffer_rsrc_t rsrc_t;

constexpr int S_ = 2048, D_ = 512, F_ = 1376;
constexpr int WS_EPOCH = 0;
constexpr int WS_FLAGZ = 1024;
constexpr int WS_FLAGA = 4096;
constexpr int WS_FLAGB = 8192;
constexpr int WS_FLAGC = 16384;
constexpr int WS_KV = 32768;
constexpr int WS_Z = WS_KV + 32 * 1024;
constexpr int WS_XST = 98304;
constexpr int WS_CSQ = 131072;
constexpr int WS_CS1 = 327680;
constexpr int WS_HS = 524288;
constexpr int WS_PQ = 1 << 20;
constexpr int WS_H = 3 << 20;
constexpr int WS_ACT = 5 << 20;
constexpr int WS_BYTES = WS_ACT + 64 * 86 * 1024;
constexpr int WS_STAMP = 16 << 20;
constexpr int OOB = 0x40000000;

constexpr int SLOT = 16384, NSLOT = 8;
constexpr int ASLOT = 28672;
constexpr int L_X = 131072;
constexpr int L_ST = 143360;
constexpr int L_CS = 144384;
constexpr int L_RED = 147456;
constexpr int L_CS1P = 157696;
constexpr int L_CS1 = 160768;
constexpr int L_B1 = 161792;
constexpr int LDS_BYTES = 163840;
#define slot_of(c) ((((c) + 6) % NSLOT) * SLOT)

constexpr int AUX_SC1 = 16;
constexpr unsigned SPIN_CAP = 1u << 16;

#define MFMA32(a, b, c) __builtin_amdgcn_mfma_f32_32x32x16_bf16(__builtin_bit_cast(bf16x8, a), __builtin_bit_cast(bf16x8, b), c, 0, 0, 0)
#define BAR() do { SB(); asm volatile("s_waitcnt lgkmcnt(0)\n\ts_barrier" ::: "memory"); SB(); } while (0)
#define WAITV(n) asm volatile("s_waitcnt vmcnt(" #n ")" ::: "memory")
#define RLX_AGENT __ATOMIC_RELAXED, __HIP_MEMORY_SCOPE_AGENT
#define LD128(rs, vo, so) __builtin_amdgcn_raw_buffer_load_b128(rs, vo, so, AUX_SC1)
#define LD64(rs, vo, so) __builtin_amdgcn_raw_buffer_load_b64(rs, vo, so, AUX_SC1)
#define ST128(v, rs, vo, so) __builtin_amdgcn_raw_buffer_store_b128(v, rs, vo, so, AUX_SC1)
#define ST64(v, rs, vo, so) __builtin_amdgcn_raw_buffer_store_b64(v, rs, vo, so, AUX_SC1)

DI float asf(unsigned u) { return __builtin_bit_cast(float, u); }
DI unsigned asu(float f) { return __builtin_bit_cast(unsigned, f); }
DI unsigned pk2(float a, float b) {
  f32x2 f = {a, b};
  return __builtin_bit_cast(unsigned, __builtin_convertvector(f, bf16x2));
}
DI float bflo(unsigned u) { return asf(u << 16); }
DI float bfhi(unsigned u) { return asf(u & 0xffff0000u); }
struct LB { unsigned a[3]; };
DI LB mkbase(unsigned a) {
  LB r; r.a[0] = a; r.a[1] = a + 65536u; r.a[2] = a + 131072u;
  asm volatile("" : "+v"(r.a[0]), "+v"(r.a[1]), "+v"(r.a[2]));
  return r;
}
DI const LAS char* lp(const LB& b, int off) { return (const LAS char*)(size_t)b.a[off >> 16] + (off & 65535); }
DI v4u tr_frag(const LB& b, int off, int dt) {
  s16x4 x = __builtin_amdgcn_ds_read_tr16_b64_v4i16((LAS s16x4*)lp(b, off));
  s16x4 y = __builtin_amdgcn_ds_read_tr16_b64_v4i16((LAS s16x4*)lp(b, off + dt));
  v2u ux = __builtin_bit_cast(v2u, x), uy = __builtin_bit_cast(v2u, y);
  return v4u{ux.x, ux.y, uy.x, uy.y};
}
DI v4u rd128(const LB& b, int off) { return *(const LAS v4u*)lp(b, off); }
DI float half_sum(float v) {
  auto r = __builtin_amdgcn_permlane32_swap(asu(v), asu(v), false, false);
  const unsigned r0 = r[0], r1 = r[1];
  return asf(r0) + asf(r1);
}
DI float row_pair_sum(float v) {
  auto r = __builtin_amdgcn_permlane16_swap(asu(v), asu(v), false, false);
  const unsigned r0 = r[0], r1 = r[1];
  return asf(r0) + asf(r1);
}
template <int CTRL> DI float dpp_add(float v) { return v + asf(__builtin_amdgcn_update_dpp(0, asu(v), CTRL, 0xf, 0xf, true)); }
DI float wave_sum(float v) {
  v = dpp_add<0xB1>(v); v = dpp_add<0x4E>(v); v = dpp_add<0x141>(v); v = dpp_add<0x140>(v);
  return half_sum(row_pair_sum(v));
}
DI bool all_lanes(bool ok) { return __builtin_amdgcn_ballot_w64(!ok) == 0ull; }
#define LAM NOPK __attribute__((always_inline))
DI f32x16 rank1(float a, float b, f32x16 c) {
  asm volatile("s_nop 7\n\ts_nop 7" : "+v"(c));
  c = __builtin_amdgcn_mfma_f32_32x32x2f32(a, b, c, 0, 0, 0);
  return c;
}
DI float phi(float t) {
  float e = __builtin_amdgcn_exp2f(t * 1.44269504088896341f);
  return (t > 0.f ? t + 1.f : e) + 1e-6f;
}
DI float mish(float t) {
  const float u = __builtin_amdgcn_exp2f(t * 1.44269504088896341f);
  const float d = u * (u + 2.f) + 2.f;
  return t * (1.f - 2.f * __builtin_amdgcn_rcpf(d));
}
DI void glds(rsrc_t rs, int ldsoff, int voff, int soff) {
  asm volatile("s_mov_b32 m0, %0\n\ts_nop 0\n\tbuffer_load_dwordx4 %1, %2, %3 offen lds" ::"s"(ldsoff), "v"(voff), "s"(rs), "s"(soff) : "memory");
}
DI u64 ld_flag(const char* ws, int off) { return __hip_atomic_load((const GAS u64*)(ws + off), RLX_AGENT); }
DI void st_flag(char* ws, int off, u64 v) { __hip_atomic_store((GAS u64*)(ws + off), v, RLX_AGENT); }
DI void wait_flags(const char* ws, int off, u64 tag) {
  for (unsigned sp = 0; sp < SPIN_CAP; ++sp) {
    const bool ok = ld_flag(ws, off) == tag;
    if (all_lanes(ok)) break;
    __builtin_amdgcn_s_sleep(1);
  }
  __builtin_amdgcn_fence(__ATOMIC_ACQUIRE, "wavefront");
}

#define STAMP(i)

struct Params {
  const char* x; const char* wqkv; const char* wo; const char* w1; const char* b1; const char* w2;
  char* out; char* ws;
};

__global__ __launch_bounds__(512, 2) NOPK void fused_block(Params p) {
  extern __shared__ __attribute__((aligned(16))) char smem[];
  LAS char* lds = (LAS char*)smem;
  const int tid = threadIdx.x, lane = tid & 63;
  const int w = __builtin_amdgcn_readfirstlane(tid >> 6);
  const int b = blockIdx.x, xcd = b & 7, loc = b >> 3;
  const int mt = (xcd >> 1) * 4 + (loc >> 3), hp = (xcd & 1) * 8 + (loc & 7);
  const int panel = xcd * 4 + (loc >> 3), mem = loc & 7;
  const int ntile = mem < 3 ? 6 : 5, tile0 = mem < 3 ? 6 * mem : 18 + 5 * (mem - 3);
  char* ws = p.ws;
  STAMP(0);
  const int h2 = lane >> 5, r32 = lane & 31;
  const int w4 = w & 3;
  const rsrc_t rw = __builtin_amdgcn_make_buffer_rsrc((void*)ws, 0, WS_BYTES, 0x00020000);
  const rsrc_t rx = __builtin_amdgcn_make_buffer_rsrc((void*)p.x, 0, S_ * D_ * 2, 0x00020000);
  const rsrc_t rq = __builtin_amdgcn_make_buffer_rsrc((void*)p.wqkv, 0, D_ * 3 * D_ * 2, 0x00020000);
  const rsrc_t r1 = __builtin_amdgcn_make_buffer_rsrc((void*)p.w1, 0, D_ * F_ * 2, 0x00020000);
  const unsigned ldsb = (unsigned)(size_t)lds;
  const int q4 = (lane & 15) >> 2;
  const int trcol = ((lane >> 4) & 1) * 32 + (lane & 3) * 8;
  const int xrow = w4 * 32 + r32;
  const int xsw = (xrow >> 1) & 7;
  LAS f32x4* xch = (LAS f32x4*)(lds + L_X);

  if (w >= 4) {
    const int lw = w4;
    const rsrc_t ro = __builtin_amdgcn_make_buffer_rsrc((void*)p.wo, 0, D_ * D_ * 2, 0x00020000);
    const rsrc_t r2 = __builtin_amdgcn_make_buffer_rsrc((void*)p.w2, 0, F_ * D_ * 2, 0x00020000);
    const int vx = (lane >> 3) * 1024 + (((lane & 7) ^ (((lw & 1) * 4 + (lane >> 4)) & 7)) * 16);
    const int vq = (lane >> 2) * 3072 + (lane & 3) * 16;
    const int vo = (lane >> 2) * 1024 + (lane & 3) * 16;
    const int v1 = (lane >> 2) * 2752 + (lane & 3) * 16;
    const int v1x = ntile == 6 ? v1 : OOB;
    auto issueA = [&](int c) LAM {
      const int sl = (c % 5) * ASLOT;
#pragma unroll
      for (int j = 0; j < 4; ++j) { const int i = lw + 4 * j; glds(rx, sl + i * 1024, vx, (mt * 128 + 8 * i) * 1024 + c * 128); }
#pragma unroll
      for (int t = 0; t < 3; ++t) glds(rq, sl + 16384 + t * 4096 + lw * 1024, vq, (c * 64 + 16 * lw) * 3072 + t * 1024 + hp * 64);
    };
    auto issueW = [&](int c) LAM {
      const int sl = slot_of(c);
      if (c < 4) {
#pragma unroll
        for (int u = 0; u < 4; ++u) glds(ro, sl + u * 4096 + lw * 1024, vo, ((u >> 1) * 256 + 64 * c + 16 * lw) * 1024 + mem * 128 + (u & 1) * 64);
      } else if (c < 16) {
        const int lt = (c - 4) >> 1, qq = (c - 4) & 1;
#pragma unroll
        for (int u = 0; u < 4; ++u) {
          const int kh = u >> 1, j = lw + 4 * (u & 1);
          const int so = (kh * 256 + qq * 128 + 16 * j) * 2752 + (tile0 + lt) * 64;
          glds(r1, sl + kh * 8192 + j * 1024, lt < 5 ? v1 : v1x, so);
        }
      } else if (c < 27) {
        const int i = c - 16;
#pragma unroll
        for (int u = 0; u < 4; ++u) glds(r2, sl + u * 4096 + lw * 1024, vo, ((u >> 1) * 688 + 64 * i + 16 * lw) * 1024 + mem * 128 + (u & 1) * 64);
      }
    };
    issueA(0); issueA(1); issueA(2); issueA(3); issueA(4);
    {
      const LB tn = mkbase(ldsb + (8 * h2 + q4) * 64 + trcol);
      LB xs[4];
#pragma unroll
      for (int s = 0; s < 4; ++s) xs[s] = mkbase(ldsb + xrow * 128 + (((2 * s + h2) ^ xsw) * 16));
      f32x16 aq = {};
      float mean = 0.f, rstd = 0.f, cs = 0.f;
#pragma unroll
      for (int c = 0; c < 8; ++c) {
        if (c == 0) WAITV(28); else if (c <= 4) WAITV(21); else if (c == 5) WAITV(18); else if (c == 6) WAITV(15); else WAITV(12);
        BAR();
        const int sl = (c % 5) * ASLOT;
        v4u xf[4], wq[4];
#pragma unroll
        for (int s = 0; s < 4; ++s) {
          xf[s] = rd128(xs[s], sl);
          wq[s] = tr_frag(tn, sl + 16384 + s * 1024, 256);
        }
        if (c == 7) {
          const f32x2 st = *(const LAS f32x2*)(lds + L_ST + xrow * 8);
          mean = st[0]; rstd = st[1];
          cs = *(const LAS float*)(lds + L_CS + r32 * 4);
        }
        SB();
        if (c >= 1 && c <= 3) issueA(c + 4);
        if (c == 4) issueW(0);
        if (c == 5) issueW(1);
        if (c == 6) issueW(2);
        SB();
#pragma unroll
        for (int s = 0; s < 4; ++s) aq = MFMA32(wq[s], xf[s], aq);
      }
      STAMP(1);
      aq = rank1(h2 ? 0.f : cs, -mean, aq);
      v4u f0, f1;
#pragma unroll
      for (int i = 0; i < 4; ++i) {
        f0[i] = pk2(phi(aq[2 * i] * rstd), phi(aq[2 * i + 1] * rstd));
        f1[i] = pk2(phi(aq[8 + 2 * i] * rstd), phi(aq[8 + 2 * i + 1] * rstd));
      }
      const int so = WS_PQ + ((mt * 4 + lw) * 32 + 2 * hp) * 1024;
      ST128(f0, rw, lane * 16, so);
      ST128(f1, rw, lane * 16, so + 1024);
    }
    BAR();
    WAITV(0);
    BAR();
    issueW(3); issueW(4); issueW(5); issueW(6); issueW(7);
    WAITV(16);
    STAMP(2);
    BAR();
    {
      f32x4 a = {0.f, 0.f, 0.f, 0.f};
      const int vo1 = lane < 48 ? lane * 16 : OOB;
#pragma unroll
      for (int i = 0; i < 8; ++i) a += __builtin_bit_cast(f32x4, LD128(rw, vo1, WS_CS1 + (8 * lw + i) * 5632 + tile0 * 128));
      if (lane < 48) *(LAS f32x4*)(lds + L_CS1P + lw * 768 + lane * 16) = a;
      if (lw == 0) {
        const rsrc_t rb = __builtin_amdgcn_make_buffer_rsrc((void*)p.b1, 0, F_ * 2, 0x00020000);
        const v2u bb = __builtin_amdgcn_raw_buffer_load_b64(rb, tile0 * 64 + lane * 8, 0, 0);
        if (lane < 48) *(LAS f32x4*)(lds + L_B1 + lane * 16) = f32x4{bflo(bb.x), bfhi(bb.x), bflo(bb.y), bfhi(bb.y)};
      }
    }
    BAR(); issueW(8); issueW(9); issueW(10); issueW(11);
    if (lane < 48) {
      const LAS float* cp = (const LAS float*)(lds + L_CS1P) + lw * 48 + lane;
      ((LAS float*)(lds + L_CS1))[lw * 48 + lane] = (cp[0] + cp[192]) + (cp[384] + cp[576]);
    }
    BAR();
    WAITV(0);
    STAMP(3);
    BAR();
    {
      const int mg = lw & 1, sh = lw >> 1;
      float s1 = 0.f, s2 = 0.f;
#pragma unroll
      for (int i = 0; i < 8; ++i) {
        const v2u t = LD64(rw, (8 * h2 + i) * 256 + r32 * 8, WS_HS + (panel * 2 + mg) * 4096);
        s1 += asf(t.x); s2 += asf(t.y);
      }
      s1 = half_sum(s1); s2 = half_sum(s2);
      const float mean = s1 * (1.f / 512.f);
      const float rstd = __builtin_amdgcn_rsqf(__builtin_fmaxf(s2 * (1.f / 512.f) - mean * mean, 0.f) + 1e-5f);
      float pre[8];
#pragma unroll
      for (int c = 0; c < 13; ++c) {
        if (c < 12) {
          if (c == 6) WAITV(18);
          if (c == 8) WAITV(18);
          BAR();
        }
        if ((c & 1) == 1) {
          const int t = c >> 1;
          f32x4 p0[2], p1[2], cs[2], bb[2];
#pragma unroll
          for (int qq = 0; qq < 2; ++qq) {
            const int q = 2 * sh + qq;
            p0[qq] = xch[(mg * 4 + q) * 64 + lane]; p1[qq] = xch[((mg + 2) * 4 + q) * 64 + lane];
            cs[qq] = *(const LAS f32x4*)(lds + L_CS1 + (t * 32 + 8 * q + 4 * h2) * 4);
            bb[qq] = *(const LAS f32x4*)(lds + L_B1 + (t * 32 + 8 * q + 4 * h2) * 4);
          }
          SB();
          if (c <= 10) issueW(13 + c);
          SB();
#pragma unroll
          for (int qq = 0; qq < 2; ++qq)
#pragma unroll
            for (int i = 0; i < 4; ++i) pre[4 * qq + i] = ((p0[qq][i] + p1[qq][i]) - mean * cs[qq][i]) * rstd + bb[qq][i];
#pragma unroll
          for (int i = 0; i < 2; ++i) pre[i] = mish(pre[i]);
        } else {
          if (c == 0) { issueW(12); issueW(13); }
          else if (c <= 10) issueW(13 + c);
          SB();
          if (c >= 2) {
            const int t = (c >> 1) - 1;
#pragma unroll
            for (int i = 2; i < 8; ++i) pre[i] = mish(pre[i]);
            const v4u af = {pk2(pre[0], pre[1]), pk2(pre[2], pre[3]), pk2(pre[4], pre[5]), pk2(pre[6], pre[7])};
            const int so = WS_ACT + ((panel * 2 + mg) * 86 + 2 * (tile0 + t) + sh) * 1024;
            ST128(af, rw, (t < ntile) ? lane * 16 : OOB, so);
          }
        }
      }
    }
    WAITV(0);
    BAR();
    STAMP(4);
    BAR();
    BAR(); issueW(24); issueW(25);
    BAR(); issueW(26);
    WAITV(0); BAR();
    BAR();
    return;
  }

  const int mg = w & 1, kh = w >> 1;
  if (mt == 0 && w == 0) {
    const v4u z4 = {0u, 0u, 0u, 0u};
    ST128(z4, rw, lane * 16, WS_KV + hp * 2048);
    ST128(z4, rw, lane * 16, WS_KV + hp * 2048 + 1024);
    ST128(z4, rw, lane < 8 ? lane * 16 : OOB, WS_Z + hp * 128);
  }
  u64 tag;
  v2u xres[4];
  {
    const u64 e = ld_flag(ws, WS_EPOCH) + 1ull;
    v4u xr[2];
#pragma unroll
    for (int i = 0; i < 2; ++i) xr[i] = __builtin_amdgcn_raw_buffer_load_b128(rx, lane * 16, (mt * 128 + 8 * hp + 2 * w + i) * 1024, 0);
    unsigned wr[8];
    const int vq2 = (w < 3) ? (lane >> 4) * 8 * 3072 + (lane & 15) * 4 : OOB;
#pragma unroll
    for (int i = 0; i < 8; ++i) wr[i] = __builtin_amdgcn_raw_buffer_load_b32(rq, vq2, (32 * mt + i) * 3072 + w * 1024 + hp * 64, 0);
    unsigned w1r[4][2];
#pragma unroll
    for (int i = 0; i < 4; ++i) {
      w1r[i][0] = __builtin_amdgcn_raw_buffer_load_b32(r1, lane * 4, (16 * panel + 4 * w + i) * 2752 + tile0 * 64, 0);
      w1r[i][1] = __builtin_amdgcn_raw_buffer_load_b32(r1, (lane < ntile * 16 - 64) ? 256 + lane * 4 : OOB, (16 * panel + 4 * w + i) * 2752 + tile0 * 64, 0);
    }
#pragma unroll
    for (int q = 0; q < 4; ++q)
      xres[q] = __builtin_amdgcn_raw_buffer_load_b64(rx, r32 * 1024 + h2 * 8 + q * 16, (panel * 64 + mg * 32) * 1024 + (mem * 64 + kh * 32) * 2, 0);
    SB();
    const unsigned tlo = __builtin_amdgcn_readfirstlane((unsigned)e), thi = __builtin_amdgcn_readfirstlane((unsigned)(e >> 32));
    tag = (u64)tlo | ((u64)thi << 32);
    float mean[2], var[2];
    float ee[2][8];
#pragma unroll
    for (int i = 0; i < 2; ++i) {
#pragma unroll
      for (int j = 0; j < 4; ++j) { ee[i][2 * j] = bflo(xr[i][j]); ee[i][2 * j + 1] = bfhi(xr[i][j]); }
      mean[i] = ((ee[i][0] + ee[i][1]) + (ee[i][2] + ee[i][3])) + ((ee[i][4] + ee[i][5]) + (ee[i][6] + ee[i][7]));
    }
    float a0 = 0.f, a1 = 0.f;
#pragma unroll
    for (int i = 0; i < 8; ++i) { a0 += bflo(wr[i]); a1 += bfhi(wr[i]); }
    a0 = row_pair_sum(a0); a1 = row_pair_sum(a1);
    a0 = half_sum(a0); a1 = half_sum(a1);
    {
      const v4u o = {asu(a0), tlo, asu(a1), thi};
      ST128(o, rw, (lane < 16 && w < 3) ? lane * 16 : OOB, WS_CSQ + ((hp * 16 + mt) * 48 + w * 16) * 16);
    }
#pragma unroll
    for (int i = 0; i < 2; ++i) mean[i] = wave_sum(mean[i]) * (1.f / 512.f);
#pragma unroll
    for (int i = 0; i < 2; ++i) {
      float v = 0.f;
#pragma unroll
      for (int j = 0; j < 8; ++j) { const float d = ee[i][j] - mean[i]; v += d * d; }
      var[i] = v;
    }
#pragma unroll
    for (int i = 0; i < 2; ++i) var[i] = wave_sum(var[i]) * (1.f / 512.f);
    {
      const bool odd = lane & 1;
      const v4u st = {asu(odd ? mean[1] : mean[0]), tlo, asu(__builtin_amdgcn_rsqf((odd ? var[1] : var[0]) + 1e-5f)), thi};
      ST128(st, rw, lane < 2 ? lane * 16 : OOB, WS_XST + (mt * 128 + 8 * hp + 2 * w) * 16);
    }
    {
      float a[4] = {0.f, 0.f, 0.f, 0.f};
#pragma unroll
      for (int i = 0; i < 4; ++i) { a[0] += bflo(w1r[i][0]); a[1] += bfhi(w1r[i][0]); a[2] += bflo(w1r[i][1]); a[3] += bfhi(w1r[i][1]); }
      LAS f32x2* t = (LAS f32x2*)(lds + L_RED);
      t[w * 128 + lane] = f32x2{a[0], a[1]};
      t[w * 128 + 64 + lane] = f32x2{a[2], a[3]};
    }
  }

  {
    const LB tn = mkbase(ldsb + (8 * h2 + q4) * 64 + trcol);
    LB xs[4];
#pragma unroll
    for (int s = 0; s < 4; ++s) xs[s] = mkbase(ldsb + xrow * 128 + (((2 * s + h2) ^ xsw) * 16));
    f32x16 ak = {}, av = {};
    u64 fz = 0;
    f32x4 rr[4];
    f32x16 ck, cv;
    v4u gst, gcs[4];
    const unsigned tlo = (unsigned)tag, thi = (unsigned)(tag >> 32);
    const int gso = WS_XST + (mt * 128 + w * 32) * 16, gco = WS_CSQ + (hp * 16 * 48 + (w < 3 ? w : 0) * 16) * 16;
    const int gcv = (lane >> 4) * 4 * 768 + (lane & 15) * 16;
#pragma unroll
    for (int c = 0; c < 8; ++c) {
      BAR();
      const int sl = (c % 5) * ASLOT;
      v4u xf[4], wk[4], wv[4];
#pragma unroll
      for (int s = 0; s < 4; ++s) {
        xf[s] = rd128(xs[s], sl);
        wk[s] = tr_frag(tn, sl + 16384 + 4096 + s * 1024, 256);
        wv[s] = tr_frag(tn, sl + 16384 + 8192 + s * 1024, 256);
      }
      SB();
#pragma unroll
      for (int s = 0; s < 4; ++s) {
        ak = MFMA32(xf[s], wk[s], ak);
        av = MFMA32(xf[s], wv[s], av);
      }
      SB();
      if (c == 0) {
        if (w < 2) {
          const LAS f32x2* t = (const LAS f32x2*)(lds + L_RED) + w * 64 + lane;
          const f32x2 a = (t[0] + t[128]) + (t[256] + t[384]);
          const v2u o = {asu(a[0]), asu(a[1])};
          ST64(o, rw, (w * 64 + lane < ntile * 16) ? w * 512 + lane * 8 : OOB, WS_CS1 + panel * 5632 + tile0 * 128);
        }
      }
      if (c == 2) {
        if (mt == 0 && w == 0) {
          WAITV(0);
          if (lane == 0) st_flag(ws, WS_FLAGZ + hp * 64, tag);
        }
      }
      if (c == 4) {
        gst = LD128(rw, r32 * 16, gso);
#pragma unroll
        for (int i = 0; i < 4; ++i) gcs[i] = LD128(rw, gcv + i * 768, gco);
      }
      if (c == 5) {
        for (unsigned sp = 0; sp < SPIN_CAP; ++sp) {
          bool ok = gst[1] == tlo && gst[3] == thi;
#pragma unroll
          for (int i = 0; i < 4; ++i) ok = ok && gcs[i][1] == tlo && gcs[i][3] == thi;
          if (all_lanes(ok)) break;
          gst = LD128(rw, r32 * 16, gso);
#pragma unroll
          for (int i = 0; i < 4; ++i) gcs[i] = LD128(rw, gcv + i * 768, gco);
        }
        float a0 = (asf(gcs[0][0]) + asf(gcs[1][0])) + (asf(gcs[2][0]) + asf(gcs[3][0]));
        float a1 = (asf(gcs[0][2]) + asf(gcs[1][2])) + (asf(gcs[2][2]) + asf(gcs[3][2]));
        a0 = row_pair_sum(a0); a1 = row_pair_sum(a1);
        a0 = half_sum(a0); a1 = half_sum(a1);
        if (lane < 32) *(LAS v2u*)(lds + L_ST + xrow * 8) = v2u{gst[0], gst[2]};
        if (w < 3 && lane < 16) *(LAS f32x2*)(lds + L_CS + w * 128 + lane * 8) = f32x2{a0, a1};
        fz = ld_flag(ws, WS_FLAGZ + hp * 64);
      }
      if (c == 6) {
        const LAS float* cp = (const LAS float*)(lds + L_CS);
        const float csk = cp[32 + r32], csv = cp[64 + r32];
#pragma unroll
        for (int q = 0; q < 4; ++q) {
          const LAS f32x4* sp = (const LAS f32x4*)(lds + L_ST + (w * 32 + 8 * q + 4 * h2) * 8);
          const f32x4 s01 = sp[0], s23 = sp[1];
          rr[q] = f32x4{s01[1], s01[3], s23[1], s23[3]};
          const float n0 = -s01[0] * s01[1], n1 = -s01[2] * s01[3], n2 = -s23[0] * s23[1], n3 = -s23[2] * s23[3];
          ck[4 * q] = n0 * csk; ck[4 * q + 1] = n1 * csk; ck[4 * q + 2] = n2 * csk; ck[4 * q + 3] = n3 * csk;
          cv[4 * q] = n0 * csv; cv[4 * q + 1] = n1 * csv; cv[4 * q + 2] = n2 * csv; cv[4 * q + 3] = n3 * csv;
        }
      }
    }
    STAMP(1);
    v4u pkf[2], vf[2];
    float zs = 0.f;
#pragma unroll
    for (int i = 0; i < 8; ++i) {
      const float r0 = rr[i >> 1][(2 * i) & 3], r1v = rr[i >> 1][(2 * i + 1) & 3];
      const float k0 = phi(ak[2 * i] * r0 + ck[2 * i]), k1 = phi(ak[2 * i + 1] * r1v + ck[2 * i + 1]);
      zs += k0 + k1;
      pkf[i >> 2][i & 3] = pk2(k0, k1);
      vf[i >> 2][i & 3] = pk2(av[2 * i] * r0 + cv[2 * i], av[2 * i + 1] * r1v + cv[2 * i + 1]);
    }
    f32x16 kv = {};
    kv = MFMA32(pkf[0], vf[0], kv);
    kv = MFMA32(pkf[1], vf[1], kv);
    LAS float* red = (LAS float*)(lds + L_RED);
    {
      const bool hi = (lane & 16) != 0;
      const int hd = hi ? 1 : 0, e = lane & 15;
#pragma unroll
      for (int pp = 0; pp < 2; ++pp) {
        f32x4 v;
#pragma unroll
        for (int i = 0; i < 4; ++i) v[i] = hi ? kv[8 + 4 * pp + i] : kv[4 * pp + i];
        *(LAS f32x4*)(red + w * 512 + hd * 256 + pp * 128 + h2 * 64 + e * 4) = v;
      }
      red[2048 + w * 64 + lane] = zs;
    }
    BAR();
    if (fz != tag) wait_flags(ws, WS_FLAGZ + hp * 64, tag);
    {
      float* kvp = (float*)(ws + WS_KV) + hp * 512;
#pragma unroll
      for (int i = 0; i < 2; ++i) {
        const int idx = tid + 256 * i;
        const float v = (red[idx] + red[512 + idx]) + (red[1024 + idx] + red[1536 + idx]);
        __hip_atomic_fetch_add((GAS float*)kvp + idx, v, RLX_AGENT);
      }
      if (tid < 32) {
        float v = 0.f;
#pragma unroll
        for (int i = 0; i < 8; ++i) v += red[2048 + i * 32 + tid];
        __hip_atomic_fetch_add((GAS float*)(ws + WS_Z) + hp * 32 + tid, v, RLX_AGENT);
      }
    }
    WAITV(0);
    BAR();
    STAMP(2);
    if (w == 0 && lane == 0) st_flag(ws, WS_FLAGA + (mt * 16 + hp) * 8, tag);
  }

  const int m32 = panel * 2 + mg;
  const LB ta = mkbase(ldsb + (4 * h2 + q4) * 64 + trcol + kh * 8192);
  v4u hres[2];
  {
    wait_flags(ws, WS_FLAGA + ((panel >> 1) * 16 + (lane & 15)) * 8, tag);
    v4u pq[16];
#pragma unroll
    for (int i = 0; i < 16; ++i) pq[i] = LD128(rw, lane * 16, WS_PQ + (m32 * 32 + 16 * kh + i) * 1024);
    if (w == 0) {
      for (unsigned sp = 0; sp < SPIN_CAP; ++sp) {
        bool ok = true;
#pragma unroll
        for (int i = 0; i < 4; ++i) ok &= (ld_flag(ws, WS_FLAGA + (lane + 64 * i) * 8) == tag);
        if (all_lanes(ok)) break;
        __builtin_amdgcn_s_sleep(1);
      }
      if (b == 0 && lane == 0) st_flag(ws, WS_EPOCH, tag);
    }
    BAR();
    STAMP(3);
    const int kvo = (r32 < 16) ? WS_KV + kh * 16384 + (h2 * 16 + r32) * 16 : WS_Z + kh * 1024 + h2 * 16;
    const int kvh = (r32 < 16) ? 1024 : 64, kvp = (r32 < 16) ? 512 : 32;
    f32x16 acc[2] = {};
    v4u kr[16][2];
#pragma unroll
    for (int i = 0; i < 12; ++i) {
      kr[i][0] = LD128(rw, kvo + i * kvh, 0);
      kr[i][1] = LD128(rw, kvo + i * kvh + kvp, 0);
    }
    v4u wf[2][2];
    wf[0][0] = tr_frag(ta, slot_of(0), 512);
    wf[0][1] = tr_frag(ta, slot_of(0) + 4096, 512);
    SB();
    f32x16 o[2];
    auto kfrag = [&](int hd) LAM {
      const f32x4 a0 = __builtin_bit_cast(f32x4, kr[hd][0]), a1 = __builtin_bit_cast(f32x4, kr[hd][1]);
      return v4u{pk2(a0[0], a0[1]), pk2(a0[2], a0[3]), pk2(a1[0], a1[1]), pk2(a1[2], a1[3])};
    };
    const f32x16 zero = {};
    o[0] = MFMA32(kfrag(0), pq[0], zero);
    v4u kfn = kfrag(1);
    SB();
#pragma unroll
    for (int hd = 0; hd < 16; ++hd) {
      if (hd + 1 < 16) {
        o[(hd + 1) & 1] = MFMA32(kfn, pq[hd + 1], zero);
        const int sl = slot_of((hd + 1) >> 2) + ((hd + 1) & 3) * 1024;
        wf[(hd + 1) & 1][0] = tr_frag(ta, sl, 512);
        wf[(hd + 1) & 1][1] = tr_frag(ta, sl + 4096, 512);
      }
      if (hd < 4) {
        kr[hd + 12][0] = LD128(rw, kvo + (hd + 12) * kvh, 0);
        kr[hd + 12][1] = LD128(rw, kvo + (hd + 12) * kvh + kvp, 0);
      }
      SB();
      const f32x16 oc = o[hd & 1];
      const float inv = __builtin_amdgcn_rcpf(oc[8] + 1e-6f);
      const v4u of = {pk2(oc[0] * inv, oc[1] * inv), pk2(oc[2] * inv, oc[3] * inv), pk2(oc[4] * inv, oc[5] * inv), pk2(oc[6] * inv, oc[7] * inv)};
      SB();
      acc[0] = MFMA32(wf[hd & 1][0], of, acc[0]);
      if (hd + 2 < 16) kfn = kfrag(hd + 2);
      SB();
      acc[1] = MFMA32(wf[hd & 1][1], of, acc[1]);
      SB();
    }
    f32x16 mine = kh ? acc[1] : acc[0], other = kh ? acc[0] : acc[1];
#pragma unroll
    for (int q = 0; q < 4; ++q) xch[(w * 4 + q) * 64 + lane] = f32x4{other[4 * q], other[4 * q + 1], other[4 * q + 2], other[4 * q + 3]};
    BAR();
    float s1 = 0.f, s2 = 0.f;
#pragma unroll
    for (int q = 0; q < 4; ++q) {
      const f32x4 t = xch[((w ^ 2) * 4 + q) * 64 + lane];
      const unsigned ua = pk2(mine[4 * q] + t[0] + bflo(xres[q].x), mine[4 * q + 1] + t[1] + bfhi(xres[q].x));
      const unsigned ub = pk2(mine[4 * q + 2] + t[2] + bflo(xres[q].y), mine[4 * q + 3] + t[3] + bfhi(xres[q].y));
      hres[q >> 1][(q & 1) * 2] = ua;
      hres[q >> 1][(q & 1) * 2 + 1] = ub;
    }
    const int so = WS_H + (m32 * 32 + 4 * mem + 2 * kh) * 1024;
    ST128(hres[0], rw, lane * 16, so);
    ST128(hres[1], rw, lane * 16, so + 1024);
    SB();
#pragma unroll
    for (int q = 0; q < 8; ++q) {
      const unsigned u = hres[q >> 2][q & 3];
      const float e0 = bflo(u), e1 = bfhi(u);
      s1 += e0 + e1;
      s2 += e0 * e0 + e1 * e1;
    }
    s1 = half_sum(s1); s2 = half_sum(s2);
    const v2u st = {asu(s1), asu(s2)};
    ST64(st, rw, lane < 32 ? lane * 8 : OOB, WS_HS + m32 * 4096 + (mem * 2 + kh) * 256);
    WAITV(0);
    BAR();
    STAMP(4);
    if (w == 0) {
      if (lane == 0) st_flag(ws, WS_FLAGB + panel * 256 + mem * 8, tag);
      wait_flags(ws, WS_FLAGB + panel * 256 + (lane & 7) * 8, tag);
    }
    BAR();
    STAMP(5);
  }

  {
    v4u hf[16];
#pragma unroll
    for (int i = 0; i < 16; ++i) hf[i] = LD128(rw, lane * 16, WS_H + (m32 * 32 + 16 * kh + i) * 1024);
    v4u R[2][8];
#pragma unroll
    for (int s = 0; s < 8; ++s) R[0][s] = tr_frag(ta, slot_of(4) + s * 1024, 512);
    SB();
    f32x16 acc;
#pragma unroll
    for (int c = 0; c < 12; ++c) {
      const int qq = c & 1;
      if (qq == 0) acc = f32x16{};
#pragma unroll
      for (int s = 0; s < 8; ++s) {
        if (c + 1 < 12) R[(c + 1) & 1][s] = tr_frag(ta, slot_of(4 + c + 1) + s * 1024, 512);
        acc = MFMA32(R[c & 1][s], hf[qq * 8 + s], acc);
        SB();
      }
      if (qq == 1) {
#pragma unroll
        for (int q = 0; q < 4; ++q) xch[(w * 4 + q) * 64 + lane] = f32x4{acc[4 * q], acc[4 * q + 1], acc[4 * q + 2], acc[4 * q + 3]};
      }
      BAR();
    }
    STAMP(6);
    BAR();
    STAMP(7);
    if (w == 0) {
      if (lane == 0) st_flag(ws, WS_FLAGC + panel * 256 + mem * 8, tag);
      wait_flags(ws, WS_FLAGC + panel * 256 + (lane & 7) * 8, tag);
    }
    BAR();
    STAMP(8);
  }

  {
    constexpr int WIN = 28;
    v4u af[WIN];
    const int aso = WS_ACT + (m32 * 86 + 43 * kh) * 1024;
#pragma unroll
    for (int i = 0; i < WIN; ++i) af[i] = LD128(rw, lane * 16, aso + i * 1024);
    v4u R[2][8];
#pragma unroll
    for (int s = 0; s < 8; ++s) R[0][s] = tr_frag(ta, slot_of(16) + (s >> 2) * 4096 + (s & 3) * 1024, 512);
    SB();
    f32x16 acc[2] = {};
#pragma unroll
    for (int c = 0; c < 11; ++c) {
#pragma unroll
      for (int s = 0; s < 4; ++s) {
        const int ks = c * 4 + s;
        if (c + 1 < 11) {
          R[(c + 1) & 1][s] = tr_frag(ta, slot_of(16 + c + 1) + s * 1024, 512);
          R[(c + 1) & 1][4 + s] = tr_frag(ta, slot_of(16 + c + 1) + 4096 + s * 1024, 512);
        }
        if (ks < 43) {
          const v4u a = af[ks % WIN];
          acc[0] = MFMA32(R[c & 1][s], a, acc[0]);
          acc[1] = MFMA32(R[c & 1][4 + s], a, acc[1]);
          if (ks + WIN < 43) af[ks % WIN] = LD128(rw, lane * 16, aso + (ks + WIN) * 1024);
        }
        SB();
      }
      if (c == 0 || c == 1 || c == 6) BAR();
    }
    f32x16 mine = kh ? acc[1] : acc[0], other = kh ? acc[0] : acc[1];
#pragma unroll
    for (int q = 0; q < 4; ++q) xch[(w * 4 + q) * 64 + lane] = f32x4{other[4 * q], other[4 * q + 1], other[4 * q + 2], other[4 * q + 3]};
    BAR();
    int lz = tid; asm volatile("" : "+v"(lz));
    const rsrc_t rout = __builtin_amdgcn_make_buffer_rsrc((void*)p.out, 0, S_ * D_ * 2, 0x00020000);
    const int oo = (lz & 31) * 1024 + ((lz >> 5) & 1) * 8;
    const int so = (panel * 64 + mg * 32) * 1024 + (mem * 64 + kh * 32) * 2;
#pragma unroll
    for (int q = 0; q < 4; ++q) {
      const f32x4 t = xch[((w ^ 2) * 4 + q) * 64 + lane];
      const unsigned ha = hres[q >> 1][(q & 1) * 2], hb = hres[q >> 1][(q & 1) * 2 + 1];
      v2u o;
      o.x = pk2(mine[4 * q] + t[0] + bflo(ha), mine[4 * q + 1] + t[1] + bfhi(ha));
      o.y = pk2(mine[4 * q + 2] + t[2] + bflo(hb), mine[4 * q + 3] + t[3] + bfhi(hb));
      __builtin_amdgcn_raw_buffer_store_b64(o, rout, oo + q * 16, so, 0);
    }
    STAMP(9);
  }
}

extern "C" void kernel_launch(void* const* d_in, const int* in_sizes, int n_in, void* d_out, int out_size, void* d_ws, size_t ws_size,
                              hipStream_t stream) {
  Params p;
  p.x = (const char*)d_in[0]; p.wqkv = (const char*)d_in[1]; p.wo = (const char*)d_in[2];
  p.w1 = (const char*)d_in[4]; p.b1 = (const char*)d_in[5]; p.w2 = (const char*)d_in[6];
  p.out = (char*)d_out; p.ws = (char*)d_ws;
  (void)hipFuncSetAttribute((const void*)fused_block, hipFuncAttributeMaxDynamicSharedMemorySize, LDS_BYTES);
  fused_block<<<256, 512, LDS_BYTES, stream>>>(p);
}
```

```cpp
#include <hip/hip_runtime.h>
#include <hip/hip_bf16.h>
#include <stdint.h>

#if defined(__HIP_DEVICE_COMPILE__)
#define NOPK __attribute__((target("no-packed-fp32-ops")))
#else
#define NOPK
#endif
#define DI __device__ __forceinline__ NOPK
#define LAM NOPK __attribute__((always_inline))
#define SB() __builtin_amdgcn_sched_barrier(0)
#define MEMB() asm volatile("" ::: "memory")
#define LAS __attribute__((address_space(3)))
#define GAS __attribute__((address_space(1)))
typedef unsigned v4u __attribute__((ext_vector_type(4)));
typedef unsigned v2u __attribute__((ext_vector_type(2)));
typedef short s16x4 __attribute__((ext_vector_type(4)));
typedef __bf16 bf16x8 __attribute__((ext_vector_type(8)));
typedef __bf16 bf16x2 __attribute__((ext_vector_type(2)));
typedef float f32x16 __attribute__((ext_vector_type(16)));
typedef float f32x4 __attribute__((ext_vector_type(4)));
typedef float f32x2 __attribute__((ext_vector_type(2)));
typedef unsigned long long u64;
typedef __amdgpu_buffer_rsrc_t rsrc_t;

constexpr int S_ = 2048, D_ = 512, F_ = 1376;
constexpr int WS_EPOCH = 0;
constexpr int WS_FLAGZ = 1024;
constexpr int WS_FLAGQ = 4096;
constexpr int WS_FLAGB = 8192;
constexpr int WS_FLAGC = 12288;
constexpr int WS_FLAGD = 16384;
constexpr int WS_KV = 32768;
constexpr int WS_Z = WS_KV + 32 * 1024;
constexpr int WS_XST = 98304;
constexpr int WS_CSQ = 131072;
constexpr int WS_FLAGA = 262144;
constexpr int WS_CS1 = 327680;
constexpr int WS_HS = 524288;
constexpr int WS_PQ = 1 << 20;
constexpr int WS_H = 3 << 20;
constexpr int WS_ACT = 5 << 20;
constexpr int WS_BYTES = WS_ACT + 64 * 86 * 1024;
constexpr int WS_STAMP = 16 << 20;
constexpr int OOB = 0x40000000;

constexpr int ASLOT = 24576;
constexpr int L_ST = 147456;
constexpr int L_CS = 147968;
constexpr int L_RED = 148992;
constexpr int SLOT = 12288, NSLOT = 10;
constexpr int L_X = 122880;
constexpr int L_KZ = L_X + 16384;
constexpr int L_ST2 = 159232;
constexpr int L_CS1 = 160256;
constexpr int L_B1 = 161024;
constexpr int L_WARM = 161792;
constexpr int L_CTL = 163584;
constexpr int C_LAND = L_CTL, C_MP = L_CTL + 16, C_HP = L_CTL + 32, C_GO = L_CTL + 48, C_CNT = L_CTL + 64, C_LAND2 = L_CTL + 96, C_TAG = L_CTL + 128;
constexpr int LDS_BYTES = 163840;
#define slot_of(c) ((((c) + 8) % NSLOT) * SLOT)
#define wa_of(c) (((c) == 10 ? 5 : (c) % 6) * ASLOT)
constexpr int LB0 = 11;
constexpr int H0 = 10;

constexpr int NSTG = 14;

#define ROT_A 1
#define WARMUP 1
#define STG 1
#define FIRSTSLEEP 8
#define MAP42 1
#define PSLEEP 2
#define LSLEEP 1
constexpr int AUX_SC1 = 16;
constexpr unsigned SPIN_CAP = 1u << 16;
constexpr unsigned LSPIN_CAP = 1u << 22;

#define MFMA32(a, b, c) __builtin_amdgcn_mfma_f32_32x32x16_bf16(__builtin_bit_cast(bf16x8, a), __builtin_bit_cast(bf16x8, b), c, 0, 0, 0)
#define WAITV_(n) asm volatile("s_waitcnt vmcnt(" #n ")" ::: "memory")
#define WAITV(n) WAITV_(n)
#define RLX_AGENT __ATOMIC_RELAXED, __HIP_MEMORY_SCOPE_AGENT
#define LD128(rs, vo, so) __builtin_amdgcn_raw_buffer_load_b128(rs, vo, so, AUX_SC1)
#define LD64(rs, vo, so) __builtin_amdgcn_raw_buffer_load_b64(rs, vo, so, AUX_SC1)
#define ST128(v, rs, vo, so) __builtin_amdgcn_raw_buffer_store_b128(v, rs, (vo) + (so), 0, AUX_SC1)
#define ST64(v, rs, vo, so) __builtin_amdgcn_raw_buffer_store_b64(v, rs, vo, so, AUX_SC1)

DI float asf(unsigned u) { return __builtin_bit_cast(float, u); }
DI unsigned asu(float f) { return __builtin_bit_cast(unsigned, f); }
DI unsigned pk2(float a, float b) {
  f32x2 f = {a, b};
  return __builtin_bit_cast(unsigned, __builtin_convertvector(f, bf16x2));
}
DI float bflo(unsigned u) { return asf(u << 16); }
DI float bfhi(unsigned u) { return asf(u & 0xffff0000u); }
struct LB { unsigned a[3]; };
DI LB mkbase(unsigned a) {
  LB r; r.a[0] = a; r.a[1] = a + 65536u; r.a[2] = a + 131072u;
  asm volatile("" : "+v"(r.a[0]), "+v"(r.a[1]), "+v"(r.a[2]));
  return r;
}
DI const LAS char* lp(const LB& b, int off) { return (const LAS char*)(size_t)b.a[off >> 16] + (off & 65535); }
DI v4u tr_frag(const LB& b, int off, int dt) {
  s16x4 x = __builtin_amdgcn_ds_read_tr16_b64_v4i16((LAS s16x4*)lp(b, off));
  s16x4 y = __builtin_amdgcn_ds_read_tr16_b64_v4i16((LAS s16x4*)lp(b, off + dt));
  v2u ux = __builtin_bit_cast(v2u, x), uy = __builtin_bit_cast(v2u, y);
  return v4u{ux.x, ux.y, uy.x, uy.y};
}
DI float half_sum(float v) {
  auto r = __builtin_amdgcn_permlane32_swap(asu(v), asu(v), false, false);
  const unsigned r0 = r[0], r1 = r[1];
  return asf(r0) + asf(r1);
}
DI float row_pair_sum(float v) {
  auto r = __builtin_amdgcn_permlane16_swap(asu(v), asu(v), false, false);
  const unsigned r0 = r[0], r1 = r[1];
  return asf(r0) + asf(r1);
}
template <int CTRL> DI float dpp_add(float v) { return v + asf(__builtin_amdgcn_update_dpp(0, asu(v), CTRL, 0xf, 0xf, true)); }
DI float wave_sum(float v) {
  v = dpp_add<0xB1>(v); v = dpp_add<0x4E>(v); v = dpp_add<0x141>(v); v = dpp_add<0x140>(v);
  return half_sum(row_pair_sum(v));
}
constexpr unsigned POISON = 0xffffffffu;
DI unsigned umax(unsigned a, unsigned b) { return a > b ? a : b; }
DI unsigned clean(unsigned d) { return d < 0xfffffffeu ? d : 0xfffffffeu; }
DI v4u clean4(v4u v) { return v4u{clean(v.x), clean(v.y), clean(v.z), clean(v.w)}; }
DI unsigned max4(v4u v) { return umax(umax(v.x, v.y), umax(v.z, v.w)); }
DI bool all_lanes(bool ok) { return __builtin_amdgcn_ballot_w64(!ok) == 0ull; }
DI f32x16 rank1(float a, float b, f32x16 c) {
  asm volatile("s_nop 7\n\ts_nop 7" : "+v"(c));
  c = __builtin_amdgcn_mfma_f32_32x32x2f32(a, b, c, 0, 0, 0);
  return c;
}
constexpr float LOG2E = 1.44269504088896341f;
DI void phi16(float (&t)[16]) {
  float e[16];
#pragma unroll
  for (int i = 0; i < 16; ++i) e[i] = __builtin_fminf(t[i], 0.f) * LOG2E;
  SB();
#pragma unroll
  for (int i = 0; i < 16; ++i) { e[i] = __builtin_amdgcn_exp2f(e[i]); t[i] = __builtin_fmaxf(t[i], 0.f) + 1e-6f; }
  SB();
#pragma unroll
  for (int i = 0; i < 16; ++i) t[i] += e[i];
}
constexpr float LN2 = 0.693147180559945309f;
template <int N> DI void mishN(float* a) {
  float u[N];
#pragma unroll
  for (int i = 0; i < N; ++i) u[i] = __builtin_amdgcn_exp2f(a[i]) + 1.f;
#pragma unroll
  for (int i = 0; i < N; ++i) u[i] = __builtin_amdgcn_rcpf(u[i] * u[i] + 1.f);
#pragma unroll
  for (int i = 0; i < N; ++i) a[i] = a[i] * (LN2 - (2.f * LN2) * u[i]);
}
DI void glds(rsrc_t rs, int ldsoff, int voff, int soff) {
  asm volatile("s_mov_b32 m0, %0\n\ts_nop 3\n\tbuffer_load_dwordx4 %1, %2, %3 offen lds" ::"s"(ldsoff), "v"(voff), "s"(rs), "s"(soff) : "memory");
}
DI u64 ld_flag(const char* ws, int off) { return __hip_atomic_load((const GAS u64*)(ws + off), RLX_AGENT); }
DI void st_flag(char* ws, int off, u64 v) { __hip_atomic_store((GAS u64*)(ws + off), v, RLX_AGENT); }
DI void wait_flags(const char* ws, int off, u64 tag) {
#pragma nounroll
  for (unsigned sp = 0; sp < SPIN_CAP; ++sp) {
    const bool ok = ld_flag(ws, off) == tag;
    if (all_lanes(ok)) break;
    __builtin_amdgcn_s_sleep(PSLEEP);
  }
  __builtin_amdgcn_fence(__ATOMIC_ACQUIRE, "wavefront");
}
DI unsigned min4(v4u v) {
  const unsigned a = v.x < v.y ? v.x : v.y, b = v.z < v.w ? v.z : v.w;
  return __builtin_amdgcn_readfirstlane(a < b ? a : b);
}
DI unsigned wait4(LAS char* lds, int off, unsigned e) {
  unsigned m = 0;
  SB();
#pragma nounroll
  for (unsigned sp = 0; sp < LSPIN_CAP; ++sp) {
    m = min4(*(volatile LAS v4u*)(lds + off));
    if (m >= e) break;
    __builtin_amdgcn_s_sleep(LSLEEP);
  }
  MEMB(); SB();
  return m;
}
DI void wait1(LAS char* lds, int off, unsigned e) {
  SB();
#pragma nounroll
  for (unsigned sp = 0; sp < LSPIN_CAP; ++sp) {
    if (__builtin_amdgcn_readfirstlane(*(volatile LAS unsigned*)(lds + off)) >= e) break;
    __builtin_amdgcn_s_sleep(LSLEEP);
  }
  MEMB(); SB();
}
DI void post(LAS char* lds, int off, unsigned v) {
  SB(); MEMB();
  *(volatile LAS unsigned*)(lds + off) = v;
  MEMB(); SB();
}
DI bool arrive_last(LAS char* lds, int off, unsigned n, int lane) {
  unsigned old = 0;
  if (lane == 0) old = __hip_atomic_fetch_add((LAS unsigned*)(lds + off), 1u, __ATOMIC_RELAXED, __HIP_MEMORY_SCOPE_WORKGROUP);
  return __builtin_amdgcn_readfirstlane(old) == n - 1;
}

#define TS()
#define TSDUMP()

struct Params {
  const char* x; const char* wqkv; const char* wo; const char* w1; const char* b1; const char* w2;
  char* out; char* ws;
};

__global__ __launch_bounds__(512, 2) NOPK void fused_block(Params p) {
  extern __shared__ __attribute__((aligned(16))) char smem[];
  LAS char* lds = (LAS char*)smem;
  const int tid = threadIdx.x, lane = tid & 63;
  const int w = __builtin_amdgcn_readfirstlane(tid >> 6);
  const int b = blockIdx.x, xcd = b & 7, loc = b >> 3;
  const int panel = (xcd >> 1) * 8 + (loc >> 2), mem = (xcd & 1) * 4 + (loc & 3);
  const int pp = panel >> 1, cm = (panel & 1) * 8 + mem;
  const int ntile = cm < 11 ? 3 : 2, tile0 = cm < 11 ? 3 * cm : 33 + 2 * (cm - 11);
  char* ws = p.ws;
  const int h2 = lane >> 5, r32 = lane & 31;
  const int w4 = w & 3;
  TS();
  const int mg = w & 1, kh = w4 >> 1;
  const int m32 = panel * 2 + mg;
  const rsrc_t rw = __builtin_amdgcn_make_buffer_rsrc((void*)ws, 0, WS_BYTES, 0x00020000);
  const rsrc_t rx = __builtin_amdgcn_make_buffer_rsrc((void*)p.x, 0, S_ * D_ * 2, 0x00020000);
  const rsrc_t rq = __builtin_amdgcn_make_buffer_rsrc((void*)p.wqkv, 0, D_ * 3 * D_ * 2, 0x00020000);
  const rsrc_t r1 = __builtin_amdgcn_make_buffer_rsrc((void*)p.w1, 0, D_ * F_ * 2, 0x00020000);
  const unsigned ldsb = (unsigned)(size_t)lds;
  const int q4 = (lane & 15) >> 2;
  const int trcol = ((lane >> 4) & 1) * 32 + (lane & 3) * 8;
  const int xrow = mg * 32 + r32;
  const int xsw = (xrow >> 1) & 7;
  LAS f32x4* xch = (LAS f32x4*)(lds + L_X);
  unsigned xs[4][2];
#pragma unroll
  for (int s = 0; s < 4; ++s) {
    xs[s][0] = ldsb + xrow * 128 + (((2 * s + h2) ^ xsw) * 16); xs[s][1] = xs[s][0] + 3 * ASLOT;
    asm volatile("" : "+v"(xs[s][0]), "+v"(xs[s][1]));
  }
  const LB tn = mkbase(ldsb + (8 * h2 + q4) * 128 + ((kh ^ (q4 >> 1)) * 64) + trcol);
  const int rot = ROT_A ? (mem + panel) & 7 : 0;

  const int lw = w4;
    const rsrc_t ro = __builtin_amdgcn_make_buffer_rsrc((void*)p.wo, 0, D_ * D_ * 2, 0x00020000);
    const rsrc_t r2 = __builtin_amdgcn_make_buffer_rsrc((void*)p.w2, 0, F_ * D_ * 2, 0x00020000);
    const int wsw = ((lane & 7) ^ (((lane >> 4) & 1) << 2)) * 16;
    const int vq = (lane >> 3) * 3072 + wsw;
    const int vo = (lane >> 3) * 1024 + wsw;
    const int v1 = (lane >> 2) * 2752 + (lane & 3) * 16;
    const int v1x = ntile == 3 ? v1 : OOB;
    auto pieceW = [&](int c, int u) LAM {
      const int sl = slot_of(c);
      if (c < 6) {
        const int q = lw + 4 * u, k2 = q >= 6 ? 1 : 0, i8 = q - 6 * k2;
        glds(ro, sl + q * 1024, (c == 5 && i8 >= 2) ? OOB : vo, (k2 * 256 + 48 * c + 8 * i8) * 1024 + mem * 128);
      } else if (c < 14) {
        glds(r1, sl + u * 4096 + lw * 1024, u < 2 ? v1 : v1x, (64 * (c - 6) + 16 * lw) * 2752 + (tile0 + u) * 64);
      }
    };
    auto issueW = [&](int c) LAM {
#pragma unroll
      for (int u = 0; u < 3; ++u) pieceW(c, u);
    };
  const int myland = C_LAND + lw * 4;

  if (w >= 4) {
    const int vx = (lane >> 3) * 1024 + (((lane & 7) ^ (((lw & 1) * 4 + (lane >> 4)) & 7)) * 16);
    auto issueA = [&](int c) LAM {
      if (c < 8) {
        const int kc = (c + rot) & 7;
#pragma unroll
        for (int j = 0; j < 2; ++j) { const int i = lw + 4 * j; glds(rx, wa_of(c) + i * 1024, vx, (panel * 64 + 8 * i) * 1024 + kc * 128); }
#pragma unroll
        for (int t = 0; t < 2; ++t)
#pragma unroll
          for (int j = 0; j < 2; ++j) {
            const int i = lw + 4 * j;
            glds(rq, wa_of(c) + 8192 + t * 8192 + i * 1024, vq, (kc * 64 + 8 * i) * 3072 + (t + 1) * 1024 + mem * 128);
          }
      } else {
#pragma unroll
        for (int t = 0; t < (c == 10 ? 2 : 3); ++t) {
          const int kc = (3 * (c - 8) + t + rot) & 7;
#pragma unroll
          for (int j = 0; j < 2; ++j) { const int i = lw + 4 * j; glds(rq, wa_of(c) + t * 8192 + i * 1024, vq, (kc * 64 + 8 * i) * 3072 + mem * 128); }
        }
      }
    };
    const int myhp = C_HP + lw * 4;
    v4u stg[NSTG][3];
    auto ldW = [&](int c) LAM {
#pragma unroll
      for (int u = 0; u < 3; ++u) {
        if (c < 14) {
          stg[(c - 10) % NSTG][u] = __builtin_amdgcn_raw_buffer_load_b128(r1, u < 2 ? v1 : v1x, (64 * (c - 6) + 16 * lw) * 2752 + (tile0 + u) * 64, 0);
        } else if (3 * (c - 14) + u < 43) {
          stg[(c - 10) % NSTG][u] = __builtin_amdgcn_raw_buffer_load_b128(r2, vo, (kh * 688 + 16 * (3 * (c - 14) + u) + 8 * mg) * 1024 + mem * 128, 0);
        }
      }
      MEMB();
    };
    const LB pw1 = mkbase(ldsb + lw * 1024 + lane * 16);
    const LB pw2 = mkbase(ldsb + kh * 6144 + mg * 1024 + lane * 16);
    auto putW = [&](int c) LAM {
#pragma unroll
      for (int u = 0; u < 3; ++u) {
        if (c < 14) *(LAS v4u*)lp(pw1, slot_of(c) + u * 4096) = stg[(c - 10) % NSTG][u];
        else if (3 * (c - 14) + u < 43) *(LAS v4u*)lp(pw2, slot_of(c) + u * 2048) = stg[(c - 10) % NSTG][u];
      }
    };
    unsigned mp = 0;
    v4u mpn;
    auto peek = [&]() LAM { mpn = *(volatile LAS v4u*)(lds + C_MP); };
    auto need = [&](unsigned e) LAM { mp = min4(mpn); if (mp < e) mp = wait4(lds, C_MP, e); };
    issueA(0);
    asm volatile("s_waitcnt lgkmcnt(0)\n\ts_barrier" ::: "memory");
    issueA(1); issueA(2);
    WAITV(12); post(lds, myland, 1); issueA(3);
    TS();
    WAITV(12); post(lds, myland, 2); issueA(4);
    WAITV(12); post(lds, myland, 3); peek(); issueA(5);
    WAITV(12); post(lds, myland, 4); need(1); peek(); issueA(6);
    WAITV(12); post(lds, myland, 5); need(2); peek(); issueA(7);
    WAITV(12); post(lds, myland, 6); need(3); peek(); issueA(8);
    WAITV(12); post(lds, myland, 7); need(4); peek(); issueA(9);
    WAITV(12); post(lds, myland, 8); need(6); peek(); issueA(10);
    TS();
    WAITV(10); post(lds, myland, 9); issueW(0); issueW(1);
    WAITV(10); post(lds, myland, 10); need(7); peek(); issueW(2); issueW(3);
    WAITV(12); post(lds, myland, 11); need(8); issueW(4); issueW(5);
    TS();
    {
      const v4u pz = {POISON, POISON, POISON, POISON};
      const int so = WS_H + (m32 * 32 + 4 * mem + 2 * kh) * 1024;
      ST128(pz, rw, lane * 16, so);
      ST128(pz, rw, lane * 16, so + 1024);
      ST128(pz, rw, lane < 16 ? lane * 16 : OOB, WS_HS + m32 * 4096 + (mem * 2 + kh) * 256);
#pragma unroll
      for (int t = 0; t < 6; ++t) ST128(pz, rw, (t < 2 * ntile) ? lane * 16 : OOB, WS_ACT + ((pp * 4 + lw) * 86 + 2 * tile0 + t) * 1024);
      MEMB();
    }
    WAITV(0); post(lds, myland, LB0 + 6);
    u64 tag;
    {
      const v2u tg = *(const LAS v2u*)(lds + C_TAG);
      const unsigned tl = __builtin_amdgcn_readfirstlane(tg.x), th = __builtin_amdgcn_readfirstlane(tg.y);
      tag = (u64)tl | ((u64)th << 32);
    }
    if (arrive_last(lds, C_CNT + 8, 8, lane)) { if (lane < 8) st_flag(ws, WS_FLAGA + lane * 8192 + b * 32, tag); }
    MEMB();
    ldW(10); ldW(11); ldW(12); ldW(13);
    wait4(lds, C_MP, 10);
    issueW(6); issueW(7); issueW(8); issueW(9);
    WAITV(0); post(lds, myland, LB0 + 10);
    TS();
    TS();
    if (lw == 2) {
      wait_flags(ws, WS_FLAGQ + panel * 128 + (lane & 7) * 8, tag);
      post(lds, C_GO + 4, 1);
    }
    if (lw == 3) {
      const int rep = WS_FLAGA + xcd * 8192;
#pragma nounroll
      for (unsigned sp = 0; sp < SPIN_CAP; ++sp) {
        bool ok = true;
#pragma unroll
        for (int i = 0; i < 4; ++i) ok &= (ld_flag(ws, rep + (lane + 64 * i) * 32) == tag);
        if (all_lanes(ok)) break;
        __builtin_amdgcn_s_sleep(PSLEEP);
      }
      post(lds, C_GO, 1);
      if (b == 0 && lane == 0) st_flag(ws, WS_EPOCH, tag);
    } else {
      wait1(lds, C_GO, 1);
    }
    TS();
    {
      f32x4 a = {0.f, 0.f, 0.f, 0.f};
      const int vo1 = (lane & 15) < 6 ? (lane >> 4) * 4 * 5632 + (lane & 15) * 16 : OOB;
#pragma unroll
      for (int i = 0; i < 4; ++i) a += __builtin_bit_cast(f32x4, LD128(rw, vo1, WS_CS1 + i * 5632 + tile0 * 128 + lw * 96));
      const rsrc_t rb = __builtin_amdgcn_make_buffer_rsrc((void*)p.b1, 0, F_ * 2, 0x00020000);
      const v2u bb = __builtin_amdgcn_raw_buffer_load_b64(rb, tile0 * 64 + lw * 48 + lane * 8, 0, 0);
#pragma unroll
      for (int i = 0; i < 4; ++i) a[i] = half_sum(row_pair_sum(a[i]));
      if (lane < 6) {
        *(LAS f32x4*)(lds + L_CS1 + lw * 96 + lane * 16) = a;
        *(LAS f32x4*)(lds + L_B1 + lw * 96 + lane * 16) = f32x4{bflo(bb.x) * LOG2E, bfhi(bb.x) * LOG2E, bflo(bb.y) * LOG2E, bfhi(bb.y) * LOG2E};
      }
      post(lds, myhp, 1);
    }
    wait4(lds, C_MP, 12);
    ldW(14); ldW(15); ldW(16); ldW(17);
    ldW(18); ldW(19); ldW(20); ldW(21); ldW(22); ldW(23);
    wait4(lds, C_MP, 14);
    putW(10); putW(11); putW(12); putW(13); putW(14); putW(15);
    post(lds, myland, LB0 + 16);
    ldW(24); ldW(25); ldW(26); ldW(27); ldW(28);
    TS();
    TS();
    {
      const int c32 = pp * 4 + lw;
      v2u hs[8];
      wait1(lds, C_MP + lw * 4, 20);
#pragma nounroll
      for (unsigned sp = 0; sp < SPIN_CAP; ++sp) {
        unsigned m = 0;
#pragma unroll
        for (int i = 0; i < 8; ++i) hs[i] = LD64(rw, (8 * h2 + i) * 256 + r32 * 8, WS_HS + c32 * 4096);
#pragma unroll
        for (int i = 0; i < 8; ++i) m = umax(m, umax(hs[i].x, hs[i].y));
        if (all_lanes(m != POISON)) break;
        MEMB();
        __builtin_amdgcn_s_sleep(PSLEEP);
      }
      float s1 = 0.f, s2 = 0.f;
#pragma unroll
      for (int i = 0; i < 8; ++i) { s1 += asf(hs[i].x); s2 += asf(hs[i].y); }
      s1 = half_sum(s1); s2 = half_sum(s2);
      const float mu = s1 * (1.f / 512.f);
      const float rstd = __builtin_amdgcn_rsqf(__builtin_fmaxf(s2 * (1.f / 512.f) - mu * mu, 0.f) + 1e-5f) * LOG2E;
      const float nm = -mu * rstd;
      if (lane < 32) *(LAS f32x2*)(lds + L_ST2 + (lw * 32 + r32) * 8) = f32x2{nm, rstd};
      post(lds, myhp, 2);
      wait4(lds, C_HP, 1);
      float o[24];
#pragma unroll
      for (int t = 0; t < 3; ++t)
#pragma unroll
        for (int qq = 0; qq < 2; ++qq) {
          const f32x4 cs = *(const LAS f32x4*)(lds + L_CS1 + (t * 32 + 16 + 8 * qq + 4 * h2) * 4);
          const f32x4 bb = *(const LAS f32x4*)(lds + L_B1 + (t * 32 + 16 + 8 * qq + 4 * h2) * 4);
#pragma unroll
          for (int i = 0; i < 4; ++i) o[8 * t + 4 * qq + i] = nm * cs[i] + bb[i];
        }
#pragma unroll
      for (int i = 0; i < 24; ++i) asm volatile("" : "+v"(o[i]));
      wait4(lds, C_MP, 21); putW(16); putW(17);
      wait4(lds, C_MP, 23); putW(18); putW(19);
      post(lds, myland, LB0 + 20);
      TS();
      wait1(lds, C_MP + lw * 4, 40);
      f32x4 pv[6];
#pragma unroll
      for (int t = 0; t < 6; ++t) pv[t] = xch[(lw * 6 + t) * 64 + lane];
      post(lds, myhp, 3);
      SB();
#pragma unroll
      for (int t = 0; t < 6; ++t)
#pragma unroll
        for (int i = 0; i < 4; ++i) o[4 * t + i] += pv[t][i] * rstd;
#pragma unroll
      for (int t = 0; t < 3; ++t) {
        mishN<8>(o + 8 * t);
        const v4u af = {pk2(o[8 * t], o[8 * t + 1]), pk2(o[8 * t + 2], o[8 * t + 3]), pk2(o[8 * t + 4], o[8 * t + 5]), pk2(o[8 * t + 6], o[8 * t + 7])};
        ST128(clean4(af), rw, (t < ntile) ? lane * 16 : OOB, WS_ACT + (c32 * 86 + 2 * (tile0 + t) + 1) * 1024);
        SB();
      }
    }
    TS();
    wait4(lds, C_MP, 40);
    putW(20); putW(21); putW(22); putW(23); post(lds, myland, LB0 + 24);
    TS();
    wait4(lds, C_MP, 54);
    putW(24); putW(25); putW(26); putW(27); putW(28); post(lds, myland, LB0 + 29);
    TS();
    TSDUMP();
    return;
  }

  const int mymp = C_MP + w * 4;
  if (tid < 32) *(LAS unsigned*)(lds + L_CTL + tid * 4) = 0u;
  if (panel == 0) {
    const v4u z4 = {0u, 0u, 0u, 0u};
    ST128(z4, rw, lane * 16, WS_KV + (mem * 4 + w) * 1024);
    ST128(z4, rw, lane < 4 ? lane * 16 : OOB, WS_Z + (mem * 4 + w) * 64);
  }
  u64 tag;
  v2u xres[4];
  const int cb = panel >> 3, rb = panel & 7;
  {
    const u64 e = ld_flag(ws, WS_EPOCH) + 1ull;
    v4u xr[2], wr[2];
#pragma unroll
    for (int i = 0; i < 2; ++i) xr[i] = __builtin_amdgcn_raw_buffer_load_b128(rx, lane * 16, (panel * 64 + 8 * mem + 2 * w + i) * 1024, 0);
    const int vq2 = (cb < 3) ? (lane >> 3) * 3072 + (lane & 7) * 16 : OOB;
#pragma unroll
    for (int i = 0; i < 2; ++i) wr[i] = __builtin_amdgcn_raw_buffer_load_b128(rq, vq2, (64 * rb + 16 * w + 8 * i) * 3072 + cb * 1024 + mem * 128, 0);
    unsigned w1r[8];
#pragma unroll
    for (int i = 0; i < 8; ++i) w1r[i] = __builtin_amdgcn_raw_buffer_load_b32(r1, lane < ntile * 16 ? lane * 4 : OOB, (32 * pp + 8 * w + i) * 2752 + tile0 * 64, 0);
    SB();
    asm volatile("s_waitcnt lgkmcnt(0)\n\ts_barrier" ::: "memory");
    SB();
    const unsigned tlo = __builtin_amdgcn_readfirstlane((unsigned)e), thi = __builtin_amdgcn_readfirstlane((unsigned)(e >> 32));
    tag = (u64)tlo | ((u64)thi << 32);
    if (tid == 0) *(LAS v2u*)(lds + C_TAG) = v2u{tlo, thi};
    float mean[2], var[2];
    float ee[2][8];
#pragma unroll
    for (int i = 0; i < 2; ++i) {
#pragma unroll
      for (int j = 0; j < 4; ++j) { ee[i][2 * j] = bflo(xr[i][j]); ee[i][2 * j + 1] = bfhi(xr[i][j]); }
      mean[i] = ((ee[i][0] + ee[i][1]) + (ee[i][2] + ee[i][3])) + ((ee[i][4] + ee[i][5]) + (ee[i][6] + ee[i][7]));
    }
#pragma unroll
    for (int i = 0; i < 2; ++i) mean[i] = wave_sum(mean[i]) * (1.f / 512.f);
#pragma unroll
    for (int i = 0; i < 2; ++i) {
      float v = 0.f;
#pragma unroll
      for (int j = 0; j < 8; ++j) { const float d = ee[i][j] - mean[i]; v += d * d; }
      var[i] = v;
    }
#pragma unroll
    for (int i = 0; i < 2; ++i) var[i] = wave_sum(var[i]) * (1.f / 512.f);
    {
      const bool odd = lane & 1;
      const v4u st = {asu(odd ? mean[1] : mean[0]), tlo, asu(__builtin_amdgcn_rsqf((odd ? var[1] : var[0]) + 1e-5f)), thi};
      ST128(st, rw, lane < 2 ? lane * 16 : OOB, WS_XST + (panel * 64 + 8 * mem + 2 * w) * 16);
    }
    {
      float a[8];
#pragma unroll
      for (int j = 0; j < 4; ++j) { a[2 * j] = bflo(wr[0][j]) + bflo(wr[1][j]); a[2 * j + 1] = bfhi(wr[0][j]) + bfhi(wr[1][j]); }
#pragma unroll
      for (int j = 0; j < 8; ++j) a[j] = half_sum(row_pair_sum(dpp_add<0x128>(a[j])));
      if (lane < 8) {
        LAS f32x4* t = (LAS f32x4*)(lds + L_RED + 4096 + w * 256 + lane * 32);
        t[0] = f32x4{a[0], a[1], a[2], a[3]}; t[1] = f32x4{a[4], a[5], a[6], a[7]};
      }
    }
    {
      float a0 = 0.f, a1 = 0.f;
#pragma unroll
      for (int i = 0; i < 8; ++i) { a0 += bflo(w1r[i]); a1 += bfhi(w1r[i]); }
      ((LAS f32x2*)(lds + L_RED))[w * 64 + lane] = f32x2{a0, a1};
    }
    if (panel == 0) WAITV(0);
    MEMB();
    if (arrive_last(lds, C_CNT + 4, 4, lane)) {
      MEMB();
      const LAS f32x2* t = (const LAS f32x2*)(lds + L_RED) + lane;
      {
        const f32x2 a = (t[0] + t[64]) + (t[128] + t[192]);
        const v2u o = {asu(a[0]), asu(a[1])};
        ST64(o, rw, (lane < ntile * 16) ? lane * 8 : OOB, WS_CS1 + pp * 5632 + tile0 * 128);
      }
      const LAS f32x2* t2 = (const LAS f32x2*)(lds + L_RED + 4096) + r32;
      const f32x2 a = (t2[0] + t2[32]) + (t2[64] + t2[96]);
      const v4u o = {asu(a[0]), tlo, asu(a[1]), thi};
      ST128(o, rw, (lane < 32 && cb < 3) ? lane * 16 : OOB, WS_CSQ + ((mem * 3 + cb) * 8 + rb) * 512);
      if (panel == 0 && lane == 0) st_flag(ws, WS_FLAGZ + mem * 64, tag);
    }
  }
  TS();

  {
    f32x16 ak = {}, av = {};
    u64 fz = 0;
    float rr[16], nn[16], csk = 0.f, csv = 0.f;
    v4u gst, gcs[4];
    const unsigned tlo = (unsigned)tag, thi = (unsigned)(tag >> 32);
    const int gco = WS_CSQ + (mem * 3 + (w < 3 ? w : 0)) * 4096;
    const int gcv = h2 * 2048 + r32 * 16;
    v4u xf[32], wk[2][2], wv[2][2];
    auto rdhalf = [&](int hc) LAM {
      const int c = hc >> 1;
#pragma unroll
      for (int i = 0; i < 2; ++i) {
        const int s = 2 * (hc & 1) + i;
        xf[4 * c + s] = *(const LAS v4u*)((const LAS char*)(size_t)xs[s][(c % 6) / 3] + (c % 3) * ASLOT);
        wk[hc & 1][i] = tr_frag(tn, wa_of(c) + 8192 + s * 2048, 512);
        wv[hc & 1][i] = tr_frag(tn, wa_of(c) + 16384 + s * 2048, 512);
      }
    };
    unsigned landed = wait4(lds, C_LAND, 1);
    TS();
    rdhalf(0);
#pragma unroll
    for (int hc = 0; hc < 16; ++hc) {
      const int c = hc >> 1;
      v4u lnext;
      if (hc + 1 < 16) {
        if ((hc & 1) && landed < (unsigned)(c + 2)) landed = wait4(lds, C_LAND, c + 2);
        rdhalf(hc + 1);
        if (!(hc & 1)) { post(lds, mymp, c + 1); lnext = *(volatile LAS v4u*)(lds + C_LAND); }
      }
      SB();
#pragma unroll
      for (int i = 0; i < 2; ++i) {
        ak = MFMA32(xf[2 * hc + i], wk[hc & 1][i], ak);
        av = MFMA32(xf[2 * hc + i], wv[hc & 1][i], av);
      }
      SB();
      if (!(hc & 1)) landed = min4(lnext);
      if (hc == 6) {
        gst = LD128(rw, lane * 16, WS_XST + panel * 1024);
#pragma unroll
        for (int i = 0; i < 4; ++i) gcs[i] = LD128(rw, gcv + i * 512, gco);
      }
      if (hc == 11) {
#pragma nounroll
        for (unsigned sp = 0; sp < SPIN_CAP; ++sp) {
          bool ok = gst[1] == tlo && gst[3] == thi;
#pragma unroll
          for (int i = 0; i < 4; ++i) ok = ok && gcs[i][1] == tlo && gcs[i][3] == thi;
          if (all_lanes(ok)) break;
          MEMB();
          gst = LD128(rw, lane * 16, WS_XST + panel * 1024);
#pragma unroll
          for (int i = 0; i < 4; ++i) gcs[i] = LD128(rw, gcv + i * 512, gco);
        }
        float a0 = (asf(gcs[0][0]) + asf(gcs[1][0])) + (asf(gcs[2][0]) + asf(gcs[3][0]));
        float a1 = (asf(gcs[0][2]) + asf(gcs[1][2])) + (asf(gcs[2][2]) + asf(gcs[3][2]));
        a0 = half_sum(a0); a1 = half_sum(a1);
        if (w == 3) *(LAS v2u*)(lds + L_ST + lane * 8) = v2u{gst[0], gst[2]};
        else if (lane < 32) *(LAS f32x2*)(lds + L_CS + w * 256 + lane * 8) = f32x2{a0, a1};
        fz = ld_flag(ws, WS_FLAGZ + mem * 64);
      }
      if (hc == 13) {
        wait4(lds, C_MP, 7);
        const LAS float* cp = (const LAS float*)(lds + L_CS);
        csk = cp[64 + kh * 32 + r32]; csv = cp[128 + kh * 32 + r32];
#pragma unroll
        for (int q = 0; q < 4; ++q) {
          const LAS f32x4* sp = (const LAS f32x4*)(lds + L_ST + (mg * 32 + 8 * q + 4 * h2) * 8);
          const f32x4 s01 = sp[0], s23 = sp[1];
          rr[4 * q] = s01[1]; rr[4 * q + 1] = s01[3]; rr[4 * q + 2] = s23[1]; rr[4 * q + 3] = s23[3];
          nn[4 * q] = -s01[0]; nn[4 * q + 1] = -s01[2]; nn[4 * q + 2] = -s23[0]; nn[4 * q + 3] = -s23[2];
        }
      }
    }
    TS();
    float kk[16];
#pragma unroll
    for (int i = 0; i < 16; ++i) kk[i] = (ak[i] + nn[i] * csk) * rr[i];
    phi16(kk);
    v4u pkf[2], vf[2];
#pragma unroll
    for (int i = 0; i < 8; ++i) {
      pkf[i >> 2][i & 3] = pk2(kk[2 * i], kk[2 * i + 1]);
      vf[i >> 2][i & 3] = pk2((av[2 * i] + nn[2 * i] * csv) * rr[2 * i], (av[2 * i + 1] + nn[2 * i + 1] * csv) * rr[2 * i + 1]);
    }
    f32x16 kv = {};
    kv = MFMA32(pkf[0], vf[0], kv);
    kv = MFMA32(pkf[1], vf[1], kv);
    const float zs = (((kk[0] + kk[1]) + (kk[2] + kk[3])) + ((kk[4] + kk[5]) + (kk[6] + kk[7]))) +
                     (((kk[8] + kk[9]) + (kk[10] + kk[11])) + ((kk[12] + kk[13]) + (kk[14] + kk[15])));
    LAS float* red = (LAS float*)(lds + L_RED);
    {
      const bool hi = (lane & 16) != 0;
      float lo8[8], hi8[8];
#pragma unroll
      for (int i = 0; i < 8; ++i) { lo8[i] = kv[i]; hi8[i] = kv[8 + i]; asm volatile("" : "+v"(lo8[i]), "+v"(hi8[i])); }
      LAS float* dst = red + mg * 1024 + kh * 512 + (hi ? 256 : 0) + h2 * 64 + (lane & 15) * 4;
#pragma unroll
      for (int pp = 0; pp < 2; ++pp) {
        f32x4 v;
#pragma unroll
        for (int i = 0; i < 4; ++i) v[i] = hi ? hi8[4 * pp + i] : lo8[4 * pp + i];
        *(LAS f32x4*)(dst + pp * 128) = v;
      }
      red[2048 + (mg * 2 + h2) * 64 + kh * 32 + r32] = zs;
    }
    post(lds, mymp, 9);
    if (fz != tag) wait_flags(ws, WS_FLAGZ + mem * 64, tag);
    wait1(lds, C_MP + (w ^ 1) * 4, 9);
    {
      float v[4];
      const int idx = (2 * kh + mg) * 256 + lane;
#pragma unroll
      for (int i = 0; i < 4; ++i) v[i] = red[idx + 64 * i] + red[1024 + idx + 64 * i];
      const int zi = kh * 32 + mg * 16 + (lane & 15);
      const float zv = (red[2048 + zi] + red[2112 + zi]) + (red[2176 + zi] + red[2240 + zi]);
      SB();
      GAS float* kvp = (GAS float*)(ws + WS_KV) + mem * 1024 + idx;
#pragma unroll
      for (int i = 0; i < 4; ++i) __hip_atomic_fetch_add(kvp + 64 * i, v[i], RLX_AGENT);
      if (lane < 16) __hip_atomic_fetch_add((GAS float*)(ws + WS_Z) + mem * 64 + zi, zv, RLX_AGENT);
    }
    TS();
    f32x16 aq = {};
    {
      wait4(lds, C_LAND, 11);
      v4u wq[2][4];
#pragma unroll
      for (int i = 0; i < 4; ++i) wq[0][i] = tr_frag(tn, wa_of(8) + i * 2048, 512);
#pragma unroll
      for (int g = 0; g < 8; ++g) {
        if (g + 1 < 8) {
#pragma unroll
          for (int i = 0; i < 4; ++i) wq[(g + 1) & 1][i] = tr_frag(tn, wa_of(8 + (g + 1) / 3) + ((g + 1) % 3) * 8192 + i * 2048, 512);
        }
        if (g == 6) post(lds, mymp, 10);
        SB();
#pragma unroll
        for (int i = 0; i < 4; ++i) aq = MFMA32(wq[g & 1][i], xf[4 * g + i], aq);
        SB();
      }
    }
    const f32x2 st = *(const LAS f32x2*)(lds + L_ST + xrow * 8);
    const float csq = *(const LAS float*)(lds + L_CS + (kh * 32 + r32) * 4);
    aq = rank1(h2 ? 0.f : csq, -st[0], aq);
    float t[16];
#pragma unroll
    for (int i = 0; i < 16; ++i) t[i] = aq[i] * st[1];
    phi16(t);
    const v4u f0 = {pk2(t[0], t[1]), pk2(t[2], t[3]), pk2(t[4], t[5]), pk2(t[6], t[7])};
    const v4u f1 = {pk2(t[8], t[9]), pk2(t[10], t[11]), pk2(t[12], t[13]), pk2(t[14], t[15])};
    SB();
    WAITV(0);
    if (arrive_last(lds, C_CNT + 8, 8, lane)) { if (lane < 8) st_flag(ws, WS_FLAGA + lane * 8192 + b * 32, tag); }
    MEMB();
    TS();
    const int so = WS_PQ + (m32 * 32 + 4 * mem + 2 * kh) * 1024;
    ST128(f0, rw, lane * 16, so);
    ST128(f1, rw, lane * 16, so + 1024);
    WAITV(0);
    if (arrive_last(lds, C_CNT, 4, lane)) { if (lane == 0) st_flag(ws, WS_FLAGQ + panel * 128 + mem * 8, tag); }
    MEMB();
#pragma unroll
    for (int q = 0; q < 4; ++q)
      xres[q] = __builtin_amdgcn_raw_buffer_load_b64(rx, r32 * 1024 + h2 * 8 + q * 16, (panel * 64 + mg * 32) * 1024 + (mem * 64 + kh * 32) * 2, 0);
    TS();
  }

  const LB ta0 = mkbase(ldsb + (4 * h2 + q4) * 128 + ((q4 >> 1) ^ kh) * 64 + trcol + kh * 6144);
  const LB ta1 = mkbase(ldsb + (4 * h2 + q4) * 128 + ((q4 >> 1) ^ kh ^ 1) * 64 + trcol + kh * 6144);
  v4u hres[2];
  {
    wait1(lds, C_GO + 4, 1);
    v4u pq[16];
#pragma unroll
    for (int i = 0; i < 16; ++i) pq[i] = LD128(rw, lane * 16, WS_PQ + (m32 * 32 + 16 * kh + i) * 1024);
    TS();
    wait4(lds, C_LAND, LB0 + 6);
    v4u wf[3][2];
#pragma unroll
    for (int i = 0; i < 2; ++i) { wf[i][0] = tr_frag(ta0, slot_of(0) + i * 2048, 1024); wf[i][1] = tr_frag(ta1, slot_of(0) + i * 2048, 1024); }
    wait1(lds, C_GO, 1);
    TS();
    {
      v4u kl[4][2], zl[2];
      const int kvv = h2 * 1024 + r32 * 16;
#pragma unroll
      for (int i = 0; i < 4; ++i) {
        kl[i][0] = LD128(rw, kvv, WS_KV + (8 * w + 2 * i) * 1024);
        kl[i][1] = LD128(rw, kvv, WS_KV + (8 * w + 2 * i) * 1024 + 512);
      }
      const int zv = lane < 16 ? (lane >> 1) * 64 + (lane & 1) * 16 : OOB;
      zl[0] = LD128(rw, zv, WS_Z + w * 512);
      zl[1] = LD128(rw, zv, WS_Z + w * 512 + 32);
      auto cv = [&](v4u x, v4u y) LAM {
        return v4u{pk2(asf(x[0]), asf(x[1])), pk2(asf(x[2]), asf(x[3])), pk2(asf(y[0]), asf(y[1])), pk2(asf(y[2]), asf(y[3]))};
      };
#pragma unroll
      for (int i = 0; i < 4; ++i) *(LAS v4u*)(lds + L_X + (8 * w + 2 * i) * 512 + lane * 16) = cv(kl[i][0], kl[i][1]);
      if (lane < 16) *(LAS v4u*)(lds + L_KZ + w * 256 + lane * 16) = cv(zl[0], zl[1]);
    }
    post(lds, mymp, 12);
    wait1(lds, C_MP + (w ^ 1) * 4, 12);
    v4u kf[16];
    {
      const int kb = (r32 < 16) ? L_X + kh * 8192 + (h2 * 16 + r32) * 16 : L_KZ + kh * 512 + h2 * 16;
      const int ks = (r32 < 16) ? 512 : 32;
#pragma unroll
      for (int i = 0; i < 16; ++i) kf[i] = *(const LAS v4u*)(lds + kb + i * ks);
    }
    post(lds, mymp, 13);
    TS();
    f32x16 acc[2] = {};
    f32x16 o[2];
    v4u of[2];
    const f32x16 zero = {};
    auto scale_lo = [&](const f32x16& oc, float& inv, v4u& dst) LAM {
      inv = __builtin_amdgcn_rcpf(oc[8] + 1e-6f);
      dst[0] = pk2(oc[0] * inv, oc[1] * inv); dst[1] = pk2(oc[2] * inv, oc[3] * inv);
    };
    auto scale_hi = [&](const f32x16& oc, float inv, v4u& dst) LAM {
      dst[2] = pk2(oc[4] * inv, oc[5] * inv); dst[3] = pk2(oc[6] * inv, oc[7] * inv);
    };
    float inv;
    o[0] = MFMA32(kf[0], pq[0], zero);
    o[1] = MFMA32(kf[1], pq[1], zero);
    SB();
    scale_lo(o[0], inv, of[0]); scale_hi(o[0], inv, of[0]);
    SB();
#pragma unroll
    for (int hd = 0; hd < 16; ++hd) {
      acc[0] = MFMA32(wf[hd % 3][0], of[hd & 1], acc[0]);
      if (hd + 1 < 16) scale_lo(o[(hd + 1) & 1], inv, of[(hd + 1) & 1]);
      SB();
      acc[1] = MFMA32(wf[hd % 3][1], of[hd & 1], acc[1]);
      if (hd + 1 < 16) scale_hi(o[(hd + 1) & 1], inv, of[(hd + 1) & 1]);
      SB();
      if (hd + 2 < 16) {
        o[hd & 1] = MFMA32(kf[hd + 2], pq[hd + 2], zero);
        const int sl = slot_of((hd + 2) / 3) + ((hd + 2) % 3) * 2048;
        wf[(hd + 2) % 3][0] = tr_frag(ta0, sl, 1024);
        wf[(hd + 2) % 3][1] = tr_frag(ta1, sl, 1024);
      }
      SB();
    }
    TS();
    wait1(lds, C_MP + (w ^ 1) * 4, 13);
#pragma unroll
    for (int q = 0; q < 4; ++q) xch[(w * 4 + q) * 64 + lane] = f32x4{acc[1][4 * q], acc[1][4 * q + 1], acc[1][4 * q + 2], acc[1][4 * q + 3]};
    post(lds, mymp, 14);
    wait1(lds, C_MP + (w ^ 2) * 4, 14);
    float s1 = 0.f, s2 = 0.f;
#pragma unroll
    for (int q = 0; q < 4; ++q) {
      const f32x4 t = xch[((w ^ 2) * 4 + q) * 64 + lane];
      const unsigned ua = pk2(acc[0][4 * q] + t[0] + bflo(xres[q].x), acc[0][4 * q + 1] + t[1] + bfhi(xres[q].x));
      const unsigned ub = pk2(acc[0][4 * q + 2] + t[2] + bflo(xres[q].y), acc[0][4 * q + 3] + t[3] + bfhi(xres[q].y));
      hres[q >> 1][(q & 1) * 2] = ua;
      hres[q >> 1][(q & 1) * 2 + 1] = ub;
    }
    const int so = WS_H + (m32 * 32 + 4 * mem + 2 * kh) * 1024;
    post(lds, mymp, 15);
    hres[0] = clean4(hres[0]); hres[1] = clean4(hres[1]);
    ST128(hres[0], rw, lane * 16, so);
    ST128(hres[1], rw, lane * 16, so + 1024);
    SB();
#pragma unroll
    for (int q = 0; q < 8; ++q) {
      const unsigned u = hres[q >> 2][q & 3];
      const float e0 = bflo(u), e1 = bfhi(u);
      s1 += e0 + e1;
      s2 += e0 * e0 + e1 * e1;
    }
    s1 = half_sum(s1); s2 = half_sum(s2);
    const v2u st = {clean(asu(s1)), clean(asu(s2))};
    ST64(st, rw, lane < 32 ? lane * 8 : OOB, WS_HS + m32 * 4096 + (mem * 2 + kh) * 256);
    TS();
  }

  {
    const LB tc = mkbase(ldsb + (4 * h2 + q4) * 64 + trcol);
    const int c32 = pp * 4 + w;
    v4u R[2][6];
    wait4(lds, C_LAND, LB0 + 14);
#pragma unroll
    for (int s = 0; s < 6; ++s) R[0][s] = tr_frag(tc, slot_of(6) + (s % 3) * 4096 + (s / 3) * 1024, 512);
    v4u hf[3][4];
    const int hso = WS_H + c32 * 32 * 1024;
    auto ldH = [&](int g) LAM {
#pragma unroll
      for (int i = 0; i < 4; ++i) hf[g % 3][i] = LD128(rw, lane * 16, hso + (4 * g + i) * 1024);
    };
    auto okH = [&](int g) LAM {
      return all_lanes(umax(umax(max4(hf[g % 3][0]), max4(hf[g % 3][1])), umax(max4(hf[g % 3][2]), max4(hf[g % 3][3]))) != POISON);
    };
    __builtin_amdgcn_s_sleep(FIRSTSLEEP);
    ldH(0); ldH(1); ldH(2);
    SB();
    f32x16 acc[3] = {};
#pragma unroll
    for (int j = 0; j < 16; ++j) {
      if ((j & 1) == 0) {
        const int g = j >> 1;
        if (!okH(g)) {
#pragma nounroll
          for (unsigned sp = 0; sp < SPIN_CAP; ++sp) {
            MEMB();
            __builtin_amdgcn_s_sleep(PSLEEP);
            ldH(g);
            if (okH(g)) break;
          }
          if (g + 1 < 8) ldH(g + 1);
          if (g + 2 < 8) ldH(g + 2);
        }
        SB();
        if (j == 0) { post(lds, mymp, 20); TS(); }
      }
#pragma unroll
      for (int s = 0; s < 6; ++s) {
        const int ks = 2 * j + s / 3, tt = s % 3;
        if (j + 1 < 16) R[(j + 1) & 1][s] = tr_frag(tc, slot_of(6 + ((j + 1) >> 1)) + tt * 4096 + (((j + 1) & 1) * 2 + s / 3) * 1024, 512);
        acc[tt] = MFMA32(R[j & 1][s], hf[(ks >> 2) % 3][ks & 3], acc[tt]);
        if (tt == 2 && ks + 12 < 32) hf[(ks >> 2) % 3][ks & 3] = LD128(rw, lane * 16, hso + (ks + 12) * 1024);
        SB();
      }
      if ((j & 3) == 2) post(lds, mymp, 20 + (j >> 1));
    }
    TS();
    wait4(lds, C_MP, 15);
#pragma unroll
    for (int t = 0; t < 3; ++t) {
      xch[(w * 6 + 2 * t) * 64 + lane] = f32x4{acc[t][8], acc[t][9], acc[t][10], acc[t][11]};
      xch[(w * 6 + 2 * t + 1) * 64 + lane] = f32x4{acc[t][12], acc[t][13], acc[t][14], acc[t][15]};
    }
    post(lds, mymp, 40);
    wait4(lds, C_HP, 2);
    {
      const f32x2 st = *(const LAS f32x2*)(lds + L_ST2 + (w * 32 + r32) * 8);
      float o[24];
#pragma unroll
      for (int t = 0; t < 3; ++t)
#pragma unroll
        for (int qq = 0; qq < 2; ++qq) {
          const f32x4 cs = *(const LAS f32x4*)(lds + L_CS1 + (t * 32 + 8 * qq + 4 * h2) * 4);
          const f32x4 bb = *(const LAS f32x4*)(lds + L_B1 + (t * 32 + 8 * qq + 4 * h2) * 4);
#pragma unroll
          for (int i = 0; i < 4; ++i) o[8 * t + 4 * qq + i] = acc[t][4 * qq + i] * st[1] + (st[0] * cs[i] + bb[i]);
        }
#pragma unroll
      for (int t = 0; t < 3; ++t) {
        mishN<8>(o + 8 * t);
        const v4u af = {pk2(o[8 * t], o[8 * t + 1]), pk2(o[8 * t + 2], o[8 * t + 3]), pk2(o[8 * t + 4], o[8 * t + 5]), pk2(o[8 * t + 6], o[8 * t + 7])};
        ST128(clean4(af), rw, (t < ntile) ? lane * 16 : OOB, WS_ACT + (c32 * 86 + 2 * (tile0 + t)) * 1024);
        SB();
      }
    }
    TS();
  }

  {
    v4u R[2][6];
    unsigned landed = wait4(lds, C_LAND, LB0 + 16);
#pragma unroll
    for (int s = 0; s < 3; ++s) { R[0][s] = tr_frag(ta0, slot_of(14) + s * 2048, 1024); R[0][3 + s] = tr_frag(ta1, slot_of(14) + s * 2048, 1024); }
    constexpr int NG = 5;
    v4u af[NG][3];
    const int aso = WS_ACT + (m32 * 86 + 43 * kh) * 1024;
    auto ldA = [&](int g) LAM {
#pragma unroll
      for (int i = 0; i < 3; ++i) if (3 * g + i < 43) af[g % NG][i] = LD128(rw, lane * 16, aso + (3 * g + i) * 1024);
    };
    auto okA = [&](int g) LAM {
      unsigned m = max4(af[g % NG][0]);
      if (g < 14) m = umax(m, umax(max4(af[g % NG][1]), max4(af[g % NG][2])));
      return all_lanes(m != POISON);
    };
    __builtin_amdgcn_s_sleep(FIRSTSLEEP);
#pragma unroll
    for (int g = 0; g < NG; ++g) ldA(g);
    SB();
    f32x16 acc[2] = {};
#pragma unroll
    for (int c = 0; c < 15; ++c) {
      if (c + 1 < 15 && landed < (unsigned)(LB0 + 14 + c + 2)) landed = wait4(lds, C_LAND, LB0 + 14 + c + 2);
      if (!okA(c)) {
#pragma nounroll
        for (unsigned sp = 0; sp < SPIN_CAP; ++sp) {
          MEMB();
          __builtin_amdgcn_s_sleep(PSLEEP);
          ldA(c);
          if (okA(c)) break;
        }
#pragma unroll
        for (int g = c + 1; g < c + NG; ++g) if (g < 15) ldA(g);
      }
      SB();
      if (c == 0) TS();
#pragma unroll
      for (int s = 0; s < 3; ++s) {
        const int ks = c * 3 + s;
        if (ks + 3 < 43) {
          R[(c + 1) & 1][s] = tr_frag(ta0, slot_of(14 + c + 1) + s * 2048, 1024);
          R[(c + 1) & 1][3 + s] = tr_frag(ta1, slot_of(14 + c + 1) + s * 2048, 1024);
        }
        if (ks < 43) {
          const v4u a = af[c % NG][s];
          acc[0] = MFMA32(R[c & 1][s], a, acc[0]);
          acc[1] = MFMA32(R[c & 1][3 + s], a, acc[1]);
          if (ks + 3 * NG < 43) af[c % NG][s] = LD128(rw, lane * 16, aso + (ks + 3 * NG) * 1024);
        }
        SB();
      }
      if (c == 3) post(lds, mymp, 54);
    }
    TS();
    wait4(lds, C_HP, 3);
#pragma unroll
    for (int q = 0; q < 4; ++q) xch[(w * 4 + q) * 64 + lane] = f32x4{acc[1][4 * q], acc[1][4 * q + 1], acc[1][4 * q + 2], acc[1][4 * q + 3]};
    post(lds, mymp, 70);
    wait1(lds, C_MP + (w ^ 2) * 4, 70);
    int lz = tid; asm volatile("" : "+v"(lz));
    const rsrc_t rout = __builtin_amdgcn_make_buffer_rsrc((void*)p.out, 0, S_ * D_ * 2, 0x00020000);
    const int oo = (lz & 31) * 1024 + ((lz >> 5) & 1) * 8;
    const int so = (panel * 64 + mg * 32) * 1024 + (mem * 64 + kh * 32) * 2;
#pragma unroll
    for (int q = 0; q < 4; ++q) {
      const f32x4 t = xch[((w ^ 2) * 4 + q) * 64 + lane];
      const unsigned ha = hres[q >> 1][(q & 1) * 2], hb = hres[q >> 1][(q & 1) * 2 + 1];
      v2u o;
      o.x = pk2(acc[0][4 * q] + t[0] + bflo(ha), acc[0][4 * q + 1] + t[1] + bfhi(ha));
      o.y = pk2(acc[0][4 * q + 2] + t[2] + bflo(hb), acc[0][4 * q + 3] + t[3] + bfhi(hb));
      __builtin_amdgcn_raw_buffer_store_b64(o, rout, oo + q * 16, so, 0);
    }
    TS();
    TSDUMP();
  }
}

extern "C" void kernel_launch(void* const* d_in, const int* in_sizes, int n_in, void* d_out, int out_size, void* d_ws, size_t ws_size,
                              hipStream_t stream) {
  Params p;
  p.x = (const char*)d_in[0]; p.wqkv = (const char*)d_in[1]; p.wo = (const char*)d_in[2];
  p.w1 = (const char*)d_in[4]; p.b1 = (const char*)d_in[5]; p.w2 = (const char*)d_in[6];
  p.out = (char*)d_out; p.ws = (char*)d_ws;
  (void)hipFuncSetAttribute((const void*)fused_block, hipFuncAttributeMaxDynamicSharedMemorySize, LDS_BYTES);
  fused_block<<<256, 512, LDS_BYTES, stream>>>(p);
}
```

```cpp
#include <hip/hip_runtime.h>
#include <hip/hip_bf16.h>
#include <stdint.h>

#if defined(__HIP_DEVICE_COMPILE__)
#define NOPK __attribute__((target("no-packed-fp32-ops")))
#else
#define NOPK
#endif
#define DI __device__ __forceinline__ NOPK
#define LAM NOPK __attribute__((always_inline))
#define SB() __builtin_amdgcn_sched_barrier(0)
#define MEMB() asm volatile("" ::: "memory")
#define LAS __attribute__((address_space(3)))
#define GAS __attribute__((address_space(1)))
typedef unsigned v4u __attribute__((ext_vector_type(4)));
typedef unsigned v2u __attribute__((ext_vector_type(2)));
typedef short s16x4 __attribute__((ext_vector_type(4)));
typedef __bf16 bf16x8 __attribute__((ext_vector_type(8)));
typedef __bf16 bf16x2 __attribute__((ext_vector_type(2)));
typedef float f32x16 __attribute__((ext_vector_type(16)));
typedef float f32x4 __attribute__((ext_vector_type(4)));
typedef float f32x2 __attribute__((ext_vector_type(2)));
typedef unsigned long long u64;
typedef __amdgpu_buffer_rsrc_t rsrc_t;

constexpr int S_ = 2048, D_ = 512, F_ = 1376;
constexpr int WS_EPOCH = 0;
constexpr int WS_FLAGZ = 1024;
constexpr int WS_FLAGQ = 4096;
constexpr int WS_FLAGB = 8192;
constexpr int WS_FLAGC = 12288;
constexpr int WS_FLAGD = 16384;
constexpr int WS_KV = 32768;
constexpr int WS_Z = WS_KV + 32 * 1024;
constexpr int WS_XST = 98304;
constexpr int WS_CSQ = 131072;
constexpr int WS_FLAGA = 262144;
constexpr int WS_CS1 = 327680;
constexpr int WS_HS = 524288;
constexpr int WS_PQ = 1 << 20;
constexpr int WS_H = 3 << 20;
constexpr int WS_ACT = 5 << 20;
constexpr int WS_BYTES = WS_ACT + 64 * 86 * 1024;
constexpr int WS_STAMP = 16 << 20;
constexpr int WS_T0 = 20480;
constexpr int OOB = 0x40000000;

constexpr int ASLOT = 24576;
constexpr int L_ST = 147456;
constexpr int L_CS = 147968;
constexpr int L_RED = 148992;
constexpr int SLOT = 12288, NSLOT = 10;
constexpr int L_X = 122880;
constexpr int L_KZ = L_X + 16384;
constexpr int L_ST2 = 159232;
constexpr int L_CS1 = 160256;
constexpr int L_B1 = 161024;
constexpr int L_WARM = 161792;
constexpr int L_CTL = 163584;
constexpr int C_LAND = L_CTL, C_MP = L_CTL + 16, C_HP = L_CTL + 32, C_GO = L_CTL + 48, C_CNT = L_CTL + 64, C_LAND2 = L_CTL + 96, C_TAG = L_CTL + 128;
constexpr int LDS_BYTES = 163840;
#define slot_of(c) ((((c) + 8) % NSLOT) * SLOT)
#define wa_of(c) (((c) == 10 ? 5 : (c) % 6) * ASLOT)
constexpr int LB0 = 11;
constexpr int H0 = 10;

constexpr int NSTG = 14;

#define ROT_A 1
#define WARMUP 1
#define STG 1
#define FIRSTSLEEP 8
#define MAP42 1
#define PSLEEP 2
#define LSLEEP 1
constexpr int AUX_SC1 = 16;
constexpr unsigned SPIN_CAP = 1u << 16;
constexpr unsigned LSPIN_CAP = 1u << 22;

#define MFMA32(a, b, c) __builtin_amdgcn_mfma_f32_32x32x16_bf16(__builtin_bit_cast(bf16x8, a), __builtin_bit_cast(bf16x8, b), c, 0, 0, 0)
#define WAITV_(n) asm volatile("s_waitcnt vmcnt(" #n ")" ::: "memory")
#define WAITV(n) WAITV_(n)
#define RLX_AGENT __ATOMIC_RELAXED, __HIP_MEMORY_SCOPE_AGENT
#define LD128(rs, vo, so) __builtin_amdgcn_raw_buffer_load_b128(rs, vo, so, AUX_SC1)
#define LD64(rs, vo, so) __builtin_amdgcn_raw_buffer_load_b64(rs, vo, so, AUX_SC1)
#define ST128(v, rs, vo, so) __builtin_amdgcn_raw_buffer_store_b128(v, rs, (vo) + (so), 0, AUX_SC1)
#define ST64(v, rs, vo, so) __builtin_amdgcn_raw_buffer_store_b64(v, rs, vo, so, AUX_SC1)

DI float asf(unsigned u) { return __builtin_bit_cast(float, u); }
DI unsigned asu(float f) { return __builtin_bit_cast(unsigned, f); }
DI unsigned pk2(float a, float b) {
  f32x2 f = {a, b};
  return __builtin_bit_cast(unsigned, __builtin_convertvector(f, bf16x2));
}
DI float bflo(unsigned u) { return asf(u << 16); }
DI float bfhi(unsigned u) { return asf(u & 0xffff0000u); }
struct LB { unsigned a[3]; };
DI LB mkbase(unsigned a) {
  LB r; r.a[0] = a; r.a[1] = a + 65536u; r.a[2] = a + 131072u;
  asm volatile("" : "+v"(r.a[0]), "+v"(r.a[1]), "+v"(r.a[2]));
  return r;
}
DI const LAS char* lp(const LB& b, int off) { return (const LAS char*)(size_t)b.a[off >> 16] + (off & 65535); }
DI v4u tr_frag(const LB& b, int off, int dt) {
  s16x4 x = __builtin_amdgcn_ds_read_tr16_b64_v4i16((LAS s16x4*)lp(b, off));
  s16x4 y = __builtin_amdgcn_ds_read_tr16_b64_v4i16((LAS s16x4*)lp(b, off + dt));
  v2u ux = __builtin_bit_cast(v2u, x), uy = __builtin_bit_cast(v2u, y);
  return v4u{ux.x, ux.y, uy.x, uy.y};
}
DI float half_sum(float v) {
  auto r = __builtin_amdgcn_permlane32_swap(asu(v), asu(v), false, false);
  const unsigned r0 = r[0], r1 = r[1];
  return asf(r0) + asf(r1);
}
DI float row_pair_sum(float v) {
  auto r = __builtin_amdgcn_permlane16_swap(asu(v), asu(v), false, false);
  const unsigned r0 = r[0], r1 = r[1];
  return asf(r0) + asf(r1);
}
template <int CTRL> DI float dpp_add(float v) { return v + asf(__builtin_amdgcn_update_dpp(0, asu(v), CTRL, 0xf, 0xf, true)); }
DI float wave_sum(float v) {
  v = dpp_add<0xB1>(v); v = dpp_add<0x4E>(v); v = dpp_add<0x141>(v); v = dpp_add<0x140>(v);
  return half_sum(row_pair_sum(v));
}
constexpr unsigned POISON = 0xffffffffu;
DI unsigned umax(unsigned a, unsigned b) { return a > b ? a : b; }
DI unsigned clean(unsigned d) { return d < 0xfffffffeu ? d : 0xfffffffeu; }
DI v4u clean4(v4u v) { return v4u{clean(v.x), clean(v.y), clean(v.z), clean(v.w)}; }
DI unsigned max4(v4u v) { return umax(umax(v.x, v.y), umax(v.z, v.w)); }
DI bool all_lanes(bool ok) { return __builtin_amdgcn_ballot_w64(!ok) == 0ull; }
DI f32x16 rank1(float a, float b, f32x16 c) {
  asm volatile("s_nop 7\n\ts_nop 7" : "+v"(c));
  c = __builtin_amdgcn_mfma_f32_32x32x2f32(a, b, c, 0, 0, 0);
  return c;
}
constexpr float LOG2E = 1.44269504088896341f;
DI void phi16(float (&t)[16]) {
  float e[16];
#pragma unroll
  for (int i = 0; i < 16; ++i) e[i] = __builtin_fminf(t[i], 0.f) * LOG2E;
  SB();
#pragma unroll
  for (int i = 0; i < 16; ++i) { e[i] = __builtin_amdgcn_exp2f(e[i]); t[i] = __builtin_fmaxf(t[i], 0.f) + 1e-6f; }
  SB();
#pragma unroll
  for (int i = 0; i < 16; ++i) t[i] += e[i];
}
constexpr float LN2 = 0.693147180559945309f;
template <int N> DI void mishN(float* a) {
  float u[N];
#pragma unroll
  for (int i = 0; i < N; ++i) u[i] = __builtin_amdgcn_exp2f(a[i]) + 1.f;
#pragma unroll
  for (int i = 0; i < N; ++i) u[i] = __builtin_amdgcn_rcpf(u[i] * u[i] + 1.f);
#pragma unroll
  for (int i = 0; i < N; ++i) a[i] = a[i] * (LN2 - (2.f * LN2) * u[i]);
}
DI void glds(rsrc_t rs, int ldsoff, int voff, int soff) {
  asm volatile("s_mov_b32 m0, %0\n\ts_nop 3\n\tbuffer_load_dwordx4 %1, %2, %3 offen lds" ::"s"(ldsoff), "v"(voff), "s"(rs), "s"(soff) : "memory");
}
DI u64 ld_flag(const char* ws, int off) { return __hip_atomic_load((const GAS u64*)(ws + off), RLX_AGENT); }
DI void st_flag(char* ws, int off, u64 v) { __hip_atomic_store((GAS u64*)(ws + off), v, RLX_AGENT); }
DI void wait_flags(const char* ws, int off, u64 tag) {
#pragma nounroll
  for (unsigned sp = 0; sp < SPIN_CAP; ++sp) {
    const bool ok = ld_flag(ws, off) == tag;
    if (all_lanes(ok)) break;
    __builtin_amdgcn_s_sleep(PSLEEP);
  }
  __builtin_amdgcn_fence(__ATOMIC_ACQUIRE, "wavefront");
}
DI unsigned min4(v4u v) {
  const unsigned a = v.x < v.y ? v.x : v.y, b = v.z < v.w ? v.z : v.w;
  return __builtin_amdgcn_readfirstlane(a < b ? a : b);
}
DI unsigned wait4(LAS char* lds, int off, unsigned e) {
  unsigned m = 0;
  SB();
#pragma nounroll
  for (unsigned sp = 0; sp < LSPIN_CAP; ++sp) {
    m = min4(*(volatile LAS v4u*)(lds + off));
    if (m >= e) break;
    __builtin_amdgcn_s_sleep(LSLEEP);
  }
  MEMB(); SB();
  return m;
}
DI void wait1(LAS char* lds, int off, unsigned e) {
  SB();
#pragma nounroll
  for (unsigned sp = 0; sp < LSPIN_CAP; ++sp) {
    if (__builtin_amdgcn_readfirstlane(*(volatile LAS unsigned*)(lds + off)) >= e) break;
    __builtin_amdgcn_s_sleep(LSLEEP);
  }
  MEMB(); SB();
}
DI void post(LAS char* lds, int off, unsigned v) {
  SB(); MEMB();
  *(volatile LAS unsigned*)(lds + off) = v;
  MEMB(); SB();
}
DI bool arrive_last(LAS char* lds, int off, unsigned n, int lane) {
  unsigned old = 0;
  if (lane == 0) old = __hip_atomic_fetch_add((LAS unsigned*)(lds + off), 1u, __ATOMIC_RELAXED, __HIP_MEMORY_SCOPE_WORKGROUP);
  return __builtin_amdgcn_readfirstlane(old) == n - 1;
}

#define TS()
#define TSDUMP()

struct Params {
  const char* x; const char* wqkv; const char* wo; const char* w1; const char* b1; const char* w2;
  char* out; char* ws;
};

__global__ __launch_bounds__(512, 2) NOPK void fused_block(Params p) {
  extern __shared__ __attribute__((aligned(16))) char smem[];
  LAS char* lds = (LAS char*)smem;
  const int tid = threadIdx.x, lane = tid & 63;
  const int w = __builtin_amdgcn_readfirstlane(tid >> 6);
  const int b = blockIdx.x, xcd = b & 7, loc = b >> 3;
  const int panel = (xcd >> 1) * 8 + (loc >> 2), mem = (xcd & 1) * 4 + (loc & 3);
  const int pp = panel >> 1, cm = (panel & 1) * 8 + mem;
  const int ntile = cm < 11 ? 3 : 2, tile0 = cm < 11 ? 3 * cm : 33 + 2 * (cm - 11);
  char* ws = p.ws;
  const u64 tstart = __builtin_amdgcn_s_memrealtime();
  const int h2 = lane >> 5, r32 = lane & 31;
  const int w4 = w & 3;
  TS();
  const int mg = w & 1, kh = w4 >> 1;
  const int m32 = panel * 2 + mg;
  const rsrc_t rw = __builtin_amdgcn_make_buffer_rsrc((void*)ws, 0, WS_BYTES, 0x00020000);
  const rsrc_t rx = __builtin_amdgcn_make_buffer_rsrc((void*)p.x, 0, S_ * D_ * 2, 0x00020000);
  const rsrc_t rq = __builtin_amdgcn_make_buffer_rsrc((void*)p.wqkv, 0, D_ * 3 * D_ * 2, 0x00020000);
  const rsrc_t r1 = __builtin_amdgcn_make_buffer_rsrc((void*)p.w1, 0, D_ * F_ * 2, 0x00020000);
  const unsigned ldsb = (unsigned)(size_t)lds;
  const int q4 = (lane & 15) >> 2;
  const int trcol = ((lane >> 4) & 1) * 32 + (lane & 3) * 8;
  const int xrow = mg * 32 + r32;
  const int xsw = (xrow >> 1) & 7;
  LAS f32x4* xch = (LAS f32x4*)(lds + L_X);
  unsigned xs[4][2];
#pragma unroll
  for (int s = 0; s < 4; ++s) {
    xs[s][0] = ldsb + xrow * 128 + (((2 * s + h2) ^ xsw) * 16); xs[s][1] = xs[s][0] + 3 * ASLOT;
    asm volatile("" : "+v"(xs[s][0]), "+v"(xs[s][1]));
  }
  const LB tn = mkbase(ldsb + (8 * h2 + q4) * 128 + ((kh ^ (q4 >> 1)) * 64) + trcol);
  const int rot = ROT_A ? (mem + panel) & 7 : 0;

  const int lw = w4;
    const rsrc_t ro = __builtin_amdgcn_make_buffer_rsrc((void*)p.wo, 0, D_ * D_ * 2, 0x00020000);
    const rsrc_t r2 = __builtin_amdgcn_make_buffer_rsrc((void*)p.w2, 0, F_ * D_ * 2, 0x00020000);
    const int wsw = ((lane & 7) ^ (((lane >> 4) & 1) << 2)) * 16;
    const int vq = (lane >> 3) * 3072 + wsw;
    const int vo = (lane >> 3) * 1024 + wsw;
    const int v1 = (lane >> 2) * 2752 + (lane & 3) * 16;
    const int v1x = ntile == 3 ? v1 : OOB;
    auto pieceW = [&](int c, int u) LAM {
      const int sl = slot_of(c);
      if (c < 6) {
        const int q = lw + 4 * u, k2 = q >= 6 ? 1 : 0, i8 = q - 6 * k2;
        glds(ro, sl + q * 1024, (c == 5 && i8 >= 2) ? OOB : vo, (k2 * 256 + 48 * c + 8 * i8) * 1024 + mem * 128);
      } else if (c < 14) {
        glds(r1, sl + u * 4096 + lw * 1024, u < 2 ? v1 : v1x, (64 * (c - 6) + 16 * lw) * 2752 + (tile0 + u) * 64);
      }
    };
    auto issueW = [&](int c) LAM {
#pragma unroll
      for (int u = 0; u < 3; ++u) pieceW(c, u);
    };
  const int myland = C_LAND + lw * 4;

  if (w >= 4) {
    const int vx = (lane >> 3) * 1024 + (((lane & 7) ^ (((lw & 1) * 4 + (lane >> 4)) & 7)) * 16);
    auto issueA = [&](int c) LAM {
      if (c < 8) {
        const int kc = (c + rot) & 7;
#pragma unroll
        for (int j = 0; j < 2; ++j) { const int i = lw + 4 * j; glds(rx, wa_of(c) + i * 1024, vx, (panel * 64 + 8 * i) * 1024 + kc * 128); }
#pragma unroll
        for (int t = 0; t < 2; ++t)
#pragma unroll
          for (int j = 0; j < 2; ++j) {
            const int i = lw + 4 * j;
            glds(rq, wa_of(c) + 8192 + t * 8192 + i * 1024, vq, (kc * 64 + 8 * i) * 3072 + (t + 1) * 1024 + mem * 128);
          }
      } else {
#pragma unroll
        for (int t = 0; t < (c == 10 ? 2 : 3); ++t) {
          const int kc = (3 * (c - 8) + t + rot) & 7;
#pragma unroll
          for (int j = 0; j < 2; ++j) { const int i = lw + 4 * j; glds(rq, wa_of(c) + t * 8192 + i * 1024, vq, (kc * 64 + 8 * i) * 3072 + mem * 128); }
        }
      }
    };
    const int myhp = C_HP + lw * 4;
    v4u stg[NSTG][3];
    auto ldW = [&](int c) LAM {
#pragma unroll
      for (int u = 0; u < 3; ++u) {
        if (c < 14) {
          stg[(c - 10) % NSTG][u] = __builtin_amdgcn_raw_buffer_load_b128(r1, u < 2 ? v1 : v1x, (64 * (c - 6) + 16 * lw) * 2752 + (tile0 + u) * 64, 0);
        } else if (3 * (c - 14) + u < 43) {
          stg[(c - 10) % NSTG][u] = __builtin_amdgcn_raw_buffer_load_b128(r2, vo, (kh * 688 + 16 * (3 * (c - 14) + u) + 8 * mg) * 1024 + mem * 128, 0);
        }
      }
      MEMB();
    };
    const LB pw1 = mkbase(ldsb + lw * 1024 + lane * 16);
    const LB pw2 = mkbase(ldsb + kh * 6144 + mg * 1024 + lane * 16);
    auto putW = [&](int c) LAM {
#pragma unroll
      for (int u = 0; u < 3; ++u) {
        if (c < 14) *(LAS v4u*)lp(pw1, slot_of(c) + u * 4096) = stg[(c - 10) % NSTG][u];
        else if (3 * (c - 14) + u < 43) *(LAS v4u*)lp(pw2, slot_of(c) + u * 2048) = stg[(c - 10) % NSTG][u];
      }
    };
    unsigned mp = 0;
    v4u mpn;
    auto peek = [&]() LAM { mpn = *(volatile LAS v4u*)(lds + C_MP); };
    auto need = [&](unsigned e) LAM { mp = min4(mpn); if (mp < e) mp = wait4(lds, C_MP, e); };
    issueA(0);
    asm volatile("s_waitcnt lgkmcnt(0)\n\ts_barrier" ::: "memory");
    issueA(1); issueA(2);
    WAITV(12); post(lds, myland, 1); issueA(3);
    TS();
    WAITV(12); post(lds, myland, 2); issueA(4);
    WAITV(12); post(lds, myland, 3); peek(); issueA(5);
    WAITV(12); post(lds, myland, 4); need(1); peek(); issueA(6);
    WAITV(12); post(lds, myland, 5); need(2); peek(); issueA(7);
    WAITV(12); post(lds, myland, 6); need(3); peek(); issueA(8);
    WAITV(12); post(lds, myland, 7); need(4); peek(); issueA(9);
    WAITV(12); post(lds, myland, 8); need(6); peek(); issueA(10);
    TS();
    WAITV(10); post(lds, myland, 9); issueW(0); issueW(1);
    WAITV(10); post(lds, myland, 10); need(7); peek(); issueW(2); issueW(3);
    WAITV(12); post(lds, myland, 11); need(8); issueW(4); issueW(5);
    TS();
    {
      const v4u pz = {POISON, POISON, POISON, POISON};
      const int so = WS_H + (m32 * 32 + 4 * mem + 2 * kh) * 1024;
      ST128(pz, rw, lane * 16, so);
      ST128(pz, rw, lane * 16, so + 1024);
      ST128(pz, rw, lane < 16 ? lane * 16 : OOB, WS_HS + m32 * 4096 + (mem * 2 + kh) * 256);
#pragma unroll
      for (int t = 0; t < 6; ++t) ST128(pz, rw, (t < 2 * ntile) ? lane * 16 : OOB, WS_ACT + ((pp * 4 + lw) * 86 + 2 * tile0 + t) * 1024);
      MEMB();
    }
    WAITV(0); post(lds, myland, LB0 + 6);
    u64 tag;
    {
      const v2u tg = *(const LAS v2u*)(lds + C_TAG);
      const unsigned tl = __builtin_amdgcn_readfirstlane(tg.x), th = __builtin_amdgcn_readfirstlane(tg.y);
      tag = (u64)tl | ((u64)th << 32);
    }
    if (arrive_last(lds, C_CNT + 8, 8, lane)) { if (lane < 8) st_flag(ws, WS_FLAGA + lane * 8192 + b * 32, tag); }
    MEMB();
    ldW(10); ldW(11); ldW(12); ldW(13);
    wait4(lds, C_MP, 10);
    issueW(6); issueW(7); issueW(8); issueW(9);
    WAITV(0); post(lds, myland, LB0 + 10);
    TS();
    TS();
    if (lw == 2) {
      wait_flags(ws, WS_FLAGQ + panel * 128 + (lane & 7) * 8, tag);
      post(lds, C_GO + 4, 1);
    }
    if (lw == 3) {
      const int rep = WS_FLAGA + xcd * 8192;
#pragma nounroll
      for (unsigned sp = 0; sp < SPIN_CAP; ++sp) {
        bool ok = true;
#pragma unroll
        for (int i = 0; i < 4; ++i) ok &= (ld_flag(ws, rep + (lane + 64 * i) * 32) == tag);
        if (all_lanes(ok)) break;
        __builtin_amdgcn_s_sleep(PSLEEP);
      }
      post(lds, C_GO, 1);
      if (b == 0 && lane == 0) st_flag(ws, WS_EPOCH, tag);
    } else {
      wait1(lds, C_GO, 1);
    }
    TS();
    {
      f32x4 a = {0.f, 0.f, 0.f, 0.f};
      const int vo1 = (lane & 15) < 6 ? (lane >> 4) * 4 * 5632 + (lane & 15) * 16 : OOB;
#pragma unroll
      for (int i = 0; i < 4; ++i) a += __builtin_bit_cast(f32x4, LD128(rw, vo1, WS_CS1 + i * 5632 + tile0 * 128 + lw * 96));
      const rsrc_t rb = __builtin_amdgcn_make_buffer_rsrc((void*)p.b1, 0, F_ * 2, 0x00020000);
      const v2u bb = __builtin_amdgcn_raw_buffer_load_b64(rb, tile0 * 64 + lw * 48 + lane * 8, 0, 0);
#pragma unroll
      for (int i = 0; i < 4; ++i) a[i] = half_sum(row_pair_sum(a[i]));
      if (lane < 6) {
        *(LAS f32x4*)(lds + L_CS1 + lw * 96 + lane * 16) = a;
        *(LAS f32x4*)(lds + L_B1 + lw * 96 + lane * 16) = f32x4{bflo(bb.x) * LOG2E, bfhi(bb.x) * LOG2E, bflo(bb.y) * LOG2E, bfhi(bb.y) * LOG2E};
      }
      post(lds, myhp, 1);
    }
    wait4(lds, C_MP, 12);
    ldW(14); ldW(15); ldW(16); ldW(17);
    ldW(18); ldW(19); ldW(20); ldW(21); ldW(22); ldW(23);
    wait4(lds, C_MP, 14);
    putW(10); putW(11); putW(12); putW(13); putW(14); putW(15);
    post(lds, myland, LB0 + 16);
    ldW(24); ldW(25); ldW(26); ldW(27); ldW(28);
    TS();
    TS();
    {
      const int c32 = pp * 4 + lw;
      v2u hs[8];
      wait1(lds, C_MP + lw * 4, 20);
#pragma nounroll
      for (unsigned sp = 0; sp < SPIN_CAP; ++sp) {
        unsigned m = 0;
#pragma unroll
        for (int i = 0; i < 8; ++i) hs[i] = LD64(rw, (8 * h2 + i) * 256 + r32 * 8, WS_HS + c32 * 4096);
#pragma unroll
        for (int i = 0; i < 8; ++i) m = umax(m, umax(hs[i].x, hs[i].y));
        if (all_lanes(m != POISON)) break;
        MEMB();
        __builtin_amdgcn_s_sleep(PSLEEP);
      }
      float s1 = 0.f, s2 = 0.f;
#pragma unroll
      for (int i = 0; i < 8; ++i) { s1 += asf(hs[i].x); s2 += asf(hs[i].y); }
      s1 = half_sum(s1); s2 = half_sum(s2);
      const float mu = s1 * (1.f / 512.f);
      const float rstd = __builtin_amdgcn_rsqf(__builtin_fmaxf(s2 * (1.f / 512.f) - mu * mu, 0.f) + 1e-5f) * LOG2E;
      const float nm = -mu * rstd;
      if (lane < 32) *(LAS f32x2*)(lds + L_ST2 + (lw * 32 + r32) * 8) = f32x2{nm, rstd};
      post(lds, myhp, 2);
      wait4(lds, C_HP, 1);
      float o[24];
#pragma unroll
      for (int t = 0; t < 3; ++t)
#pragma unroll
        for (int qq = 0; qq < 2; ++qq) {
          const f32x4 cs = *(const LAS f32x4*)(lds + L_CS1 + (t * 32 + 16 + 8 * qq + 4 * h2) * 4);
          const f32x4 bb = *(const LAS f32x4*)(lds + L_B1 + (t * 32 + 16 + 8 * qq + 4 * h2) * 4);
#pragma unroll
          for (int i = 0; i < 4; ++i) o[8 * t + 4 * qq + i] = nm * cs[i] + bb[i];
        }
#pragma unroll
      for (int i = 0; i < 24; ++i) asm volatile("" : "+v"(o[i]));
      wait4(lds, C_MP, 21); putW(16); putW(17);
      wait4(lds, C_MP, 23); putW(18); putW(19);
      post(lds, myland, LB0 + 20);
      TS();
      wait1(lds, C_MP + lw * 4, 40);
      f32x4 pv[6];
#pragma unroll
      for (int t = 0; t < 6; ++t) pv[t] = xch[(lw * 6 + t) * 64 + lane];
      post(lds, myhp, 3);
      SB();
#pragma unroll
      for (int t = 0; t < 6; ++t)
#pragma unroll
        for (int i = 0; i < 4; ++i) o[4 * t + i] += pv[t][i] * rstd;
#pragma unroll
      for (int t = 0; t < 3; ++t) {
        mishN<8>(o + 8 * t);
        const v4u af = {pk2(o[8 * t], o[8 * t + 1]), pk2(o[8 * t + 2], o[8 * t + 3]), pk2(o[8 * t + 4], o[8 * t + 5]), pk2(o[8 * t + 6], o[8 * t + 7])};
        ST128(clean4(af), rw, (t < ntile) ? lane * 16 : OOB, WS_ACT + (c32 * 86 + 2 * (tile0 + t) + 1) * 1024);
        SB();
      }
    }
    TS();
    wait4(lds, C_MP, 40);
    putW(20); putW(21); putW(22); putW(23); post(lds, myland, LB0 + 24);
    TS();
    wait4(lds, C_MP, 54);
    putW(24); putW(25); putW(26); putW(27); putW(28); post(lds, myland, LB0 + 29);
    TS();
    TSDUMP();
    return;
  }

  const int mymp = C_MP + w * 4;
  if (tid < 32) *(LAS unsigned*)(lds + L_CTL + tid * 4) = 0u;
  if (tid == 0) st_flag(ws, WS_T0 + b * 8, tstart);
  if (panel == 0) {
    const v4u z4 = {0u, 0u, 0u, 0u};
    ST128(z4, rw, lane * 16, WS_KV + (mem * 4 + w) * 1024);
    ST128(z4, rw, lane < 4 ? lane * 16 : OOB, WS_Z + (mem * 4 + w) * 64);
  }
  u64 tag;
  v2u xres[4];
  const int cb = panel >> 3, rb = panel & 7;
  {
    const u64 e = ld_flag(ws, WS_EPOCH) + 1ull;
    v4u xr[2], wr[2];
#pragma unroll
    for (int i = 0; i < 2; ++i) xr[i] = __builtin_amdgcn_raw_buffer_load_b128(rx, lane * 16, (panel * 64 + 8 * mem + 2 * w + i) * 1024, 0);
    const int vq2 = (cb < 3) ? (lane >> 3) * 3072 + (lane & 7) * 16 : OOB;
#pragma unroll
    for (int i = 0; i < 2; ++i) wr[i] = __builtin_amdgcn_raw_buffer_load_b128(rq, vq2, (64 * rb + 16 * w + 8 * i) * 3072 + cb * 1024 + mem * 128, 0);
    unsigned w1r[8];
#pragma unroll
    for (int i = 0; i < 8; ++i) w1r[i] = __builtin_amdgcn_raw_buffer_load_b32(r1, lane < ntile * 16 ? lane * 4 : OOB, (32 * pp + 8 * w + i) * 2752 + tile0 * 64, 0);
    SB();
    asm volatile("s_waitcnt lgkmcnt(0)\n\ts_barrier" ::: "memory");
    SB();
    const unsigned tlo = __builtin_amdgcn_readfirstlane((unsigned)e), thi = __builtin_amdgcn_readfirstlane((unsigned)(e >> 32));
    tag = (u64)tlo | ((u64)thi << 32);
    if (tid == 0) *(LAS v2u*)(lds + C_TAG) = v2u{tlo, thi};
    float mean[2], var[2];
    float ee[2][8];
#pragma unroll
    for (int i = 0; i < 2; ++i) {
#pragma unroll
      for (int j = 0; j < 4; ++j) { ee[i][2 * j] = bflo(xr[i][j]); ee[i][2 * j + 1] = bfhi(xr[i][j]); }
      mean[i] = ((ee[i][0] + ee[i][1]) + (ee[i][2] + ee[i][3])) + ((ee[i][4] + ee[i][5]) + (ee[i][6] + ee[i][7]));
    }
#pragma unroll
    for (int i = 0; i < 2; ++i) mean[i] = wave_sum(mean[i]) * (1.f / 512.f);
#pragma unroll
    for (int i = 0; i < 2; ++i) {
      float v = 0.f;
#pragma unroll
      for (int j = 0; j < 8; ++j) { const float d = ee[i][j] - mean[i]; v += d * d; }
      var[i] = v;
    }
#pragma unroll
    for (int i = 0; i < 2; ++i) var[i] = wave_sum(var[i]) * (1.f / 512.f);
    {
      const bool odd = lane & 1;
      const v4u st = {asu(odd ? mean[1] : mean[0]), tlo, asu(__builtin_amdgcn_rsqf((odd ? var[1] : var[0]) + 1e-5f)), thi};
      ST128(st, rw, lane < 2 ? lane * 16 : OOB, WS_XST + (panel * 64 + 8 * mem + 2 * w) * 16);
    }
    {
      float a[8];
#pragma unroll
      for (int j = 0; j < 4; ++j) { a[2 * j] = bflo(wr[0][j]) + bflo(wr[1][j]); a[2 * j + 1] = bfhi(wr[0][j]) + bfhi(wr[1][j]); }
#pragma unroll
      for (int j = 0; j < 8; ++j) a[j] = half_sum(row_pair_sum(dpp_add<0x128>(a[j])));
      if (lane < 8) {
        LAS f32x4* t = (LAS f32x4*)(lds + L_RED + 4096 + w * 256 + lane * 32);
        t[0] = f32x4{a[0], a[1], a[2], a[3]}; t[1] = f32x4{a[4], a[5], a[6], a[7]};
      }
    }
    {
      float a0 = 0.f, a1 = 0.f;
#pragma unroll
      for (int i = 0; i < 8; ++i) { a0 += bflo(w1r[i]); a1 += bfhi(w1r[i]); }
      ((LAS f32x2*)(lds + L_RED))[w * 64 + lane] = f32x2{a0, a1};
    }
    if (panel == 0) WAITV(0);
    MEMB();
    if (arrive_last(lds, C_CNT + 4, 4, lane)) {
      MEMB();
      const LAS f32x2* t = (const LAS f32x2*)(lds + L_RED) + lane;
      {
        const f32x2 a = (t[0] + t[64]) + (t[128] + t[192]);
        const v2u o = {asu(a[0]), asu(a[1])};
        ST64(o, rw, (lane < ntile * 16) ? lane * 8 : OOB, WS_CS1 + pp * 5632 + tile0 * 128);
      }
      const LAS f32x2* t2 = (const LAS f32x2*)(lds + L_RED + 4096) + r32;
      const f32x2 a = (t2[0] + t2[32]) + (t2[64] + t2[96]);
      const v4u o = {asu(a[0]), tlo, asu(a[1]), thi};
      ST128(o, rw, (lane < 32 && cb < 3) ? lane * 16 : OOB, WS_CSQ + ((mem * 3 + cb) * 8 + rb) * 512);
      if (panel == 0 && lane == 0) st_flag(ws, WS_FLAGZ + mem * 64, tag);
    }
  }
  TS();

  {
    f32x16 ak = {}, av = {};
    u64 fz = 0;
    float rr[16], nn[16], csk = 0.f, csv = 0.f;
    v4u gst, gcs[4];
    const unsigned tlo = (unsigned)tag, thi = (unsigned)(tag >> 32);
    const int gco = WS_CSQ + (mem * 3 + (w < 3 ? w : 0)) * 4096;
    const int gcv = h2 * 2048 + r32 * 16;
    v4u xf[32], wk[2][2], wv[2][2];
    auto rdhalf = [&](int hc) LAM {
      const int c = hc >> 1;
#pragma unroll
      for (int i = 0; i < 2; ++i) {
        const int s = 2 * (hc & 1) + i;
        xf[4 * c + s] = *(const LAS v4u*)((const LAS char*)(size_t)xs[s][(c % 6) / 3] + (c % 3) * ASLOT);
        wk[hc & 1][i] = tr_frag(tn, wa_of(c) + 8192 + s * 2048, 512);
        wv[hc & 1][i] = tr_frag(tn, wa_of(c) + 16384 + s * 2048, 512);
      }
    };
    unsigned landed = wait4(lds, C_LAND, 1);
    TS();
    rdhalf(0);
#pragma unroll
    for (int hc = 0; hc < 16; ++hc) {
      const int c = hc >> 1;
      v4u lnext;
      if (hc + 1 < 16) {
        if ((hc & 1) && landed < (unsigned)(c + 2)) landed = wait4(lds, C_LAND, c + 2);
        rdhalf(hc + 1);
        if (!(hc & 1)) { post(lds, mymp, c + 1); lnext = *(volatile LAS v4u*)(lds + C_LAND); }
      }
      SB();
#pragma unroll
      for (int i = 0; i < 2; ++i) {
        ak = MFMA32(xf[2 * hc + i], wk[hc & 1][i], ak);
        av = MFMA32(xf[2 * hc + i], wv[hc & 1][i], av);
      }
      SB();
      if (!(hc & 1)) landed = min4(lnext);
      if (hc == 6) {
        gst = LD128(rw, lane * 16, WS_XST + panel * 1024);
#pragma unroll
        for (int i = 0; i < 4; ++i) gcs[i] = LD128(rw, gcv + i * 512, gco);
      }
      if (hc == 11) {
#pragma nounroll
        for (unsigned sp = 0; sp < SPIN_CAP; ++sp) {
          bool ok = gst[1] == tlo && gst[3] == thi;
#pragma unroll
          for (int i = 0; i < 4; ++i) ok = ok && gcs[i][1] == tlo && gcs[i][3] == thi;
          if (all_lanes(ok)) break;
          MEMB();
          gst = LD128(rw, lane * 16, WS_XST + panel * 1024);
#pragma unroll
          for (int i = 0; i < 4; ++i) gcs[i] = LD128(rw, gcv + i * 512, gco);
        }
        float a0 = (asf(gcs[0][0]) + asf(gcs[1][0])) + (asf(gcs[2][0]) + asf(gcs[3][0]));
        float a1 = (asf(gcs[0][2]) + asf(gcs[1][2])) + (asf(gcs[2][2]) + asf(gcs[3][2]));
        a0 = half_sum(a0); a1 = half_sum(a1);
        if (w == 3) *(LAS v2u*)(lds + L_ST + lane * 8) = v2u{gst[0], gst[2]};
        else if (lane < 32) *(LAS f32x2*)(lds + L_CS + w * 256 + lane * 8) = f32x2{a0, a1};
        fz = ld_flag(ws, WS_FLAGZ + mem * 64);
      }
      if (hc == 13) {
        wait4(lds, C_MP, 7);
        const LAS float* cp = (const LAS float*)(lds + L_CS);
        csk = cp[64 + kh * 32 + r32]; csv = cp[128 + kh * 32 + r32];
#pragma unroll
        for (int q = 0; q < 4; ++q) {
          const LAS f32x4* sp = (const LAS f32x4*)(lds + L_ST + (mg * 32 + 8 * q + 4 * h2) * 8);
          const f32x4 s01 = sp[0], s23 = sp[1];
          rr[4 * q] = s01[1]; rr[4 * q + 1] = s01[3]; rr[4 * q + 2] = s23[1]; rr[4 * q + 3] = s23[3];
          nn[4 * q] = -s01[0]; nn[4 * q + 1] = -s01[2]; nn[4 * q + 2] = -s23[0]; nn[4 * q + 3] = -s23[2];
        }
      }
    }
    TS();
    float kk[16];
#pragma unroll
    for (int i = 0; i < 16; ++i) kk[i] = (ak[i] + nn[i] * csk) * rr[i];
    phi16(kk);
    v4u pkf[2], vf[2];
#pragma unroll
    for (int i = 0; i < 8; ++i) {
      pkf[i >> 2][i & 3] = pk2(kk[2 * i], kk[2 * i + 1]);
      vf[i >> 2][i & 3] = pk2((av[2 * i] + nn[2 * i] * csv) * rr[2 * i], (av[2 * i + 1] + nn[2 * i + 1] * csv) * rr[2 * i + 1]);
    }
    f32x16 kv = {};
    kv = MFMA32(pkf[0], vf[0], kv);
    kv = MFMA32(pkf[1], vf[1], kv);
    const float zs = (((kk[0] + kk[1]) + (kk[2] + kk[3])) + ((kk[4] + kk[5]) + (kk[6] + kk[7]))) +
                     (((kk[8] + kk[9]) + (kk[10] + kk[11])) + ((kk[12] + kk[13]) + (kk[14] + kk[15])));
    LAS float* red = (LAS float*)(lds + L_RED);
    {
      const bool hi = (lane & 16) != 0;
      float lo8[8], hi8[8];
#pragma unroll
      for (int i = 0; i < 8; ++i) { lo8[i] = kv[i]; hi8[i] = kv[8 + i]; asm volatile("" : "+v"(lo8[i]), "+v"(hi8[i])); }
      LAS float* dst = red + mg * 1024 + kh * 512 + (hi ? 256 : 0) + h2 * 64 + (lane & 15) * 4;
#pragma unroll
      for (int pp = 0; pp < 2; ++pp) {
        f32x4 v;
#pragma unroll
        for (int i = 0; i < 4; ++i) v[i] = hi ? hi8[4 * pp + i] : lo8[4 * pp + i];
        *(LAS f32x4*)(dst + pp * 128) = v;
      }
      red[2048 + (mg * 2 + h2) * 64 + kh * 32 + r32] = zs;
    }
    post(lds, mymp, 9);
    if (fz != tag) wait_flags(ws, WS_FLAGZ + mem * 64, tag);
    wait1(lds, C_MP + (w ^ 1) * 4, 9);
    {
      float v[4];
      const int idx = (2 * kh + mg) * 256 + lane;
#pragma unroll
      for (int i = 0; i < 4; ++i) v[i] = red[idx + 64 * i] + red[1024 + idx + 64 * i];
      const int zi = kh * 32 + mg * 16 + (lane & 15);
      const float zv = (red[2048 + zi] + red[2112 + zi]) + (red[2176 + zi] + red[2240 + zi]);
      SB();
      GAS float* kvp = (GAS float*)(ws + WS_KV) + mem * 1024 + idx;
#pragma unroll
      for (int i = 0; i < 4; ++i) __hip_atomic_fetch_add(kvp + 64 * i, v[i], RLX_AGENT);
      if (lane < 16) __hip_atomic_fetch_add((GAS float*)(ws + WS_Z) + mem * 64 + zi, zv, RLX_AGENT);
    }
    TS();
    f32x16 aq = {};
    {
      wait4(lds, C_LAND, 11);
      v4u wq[2][4];
#pragma unroll
      for (int i = 0; i < 4; ++i) wq[0][i] = tr_frag(tn, wa_of(8) + i * 2048, 512);
#pragma unroll
      for (int g = 0; g < 8; ++g) {
        if (g + 1 < 8) {
#pragma unroll
          for (int i = 0; i < 4; ++i) wq[(g + 1) & 1][i] = tr_frag(tn, wa_of(8 + (g + 1) / 3) + ((g + 1) % 3) * 8192 + i * 2048, 512);
        }
        if (g == 6) post(lds, mymp, 10);
        SB();
#pragma unroll
        for (int i = 0; i < 4; ++i) aq = MFMA32(wq[g & 1][i], xf[4 * g + i], aq);
        SB();
      }
    }
    const f32x2 st = *(const LAS f32x2*)(lds + L_ST + xrow * 8);
    const float csq = *(const LAS float*)(lds + L_CS + (kh * 32 + r32) * 4);
    aq = rank1(h2 ? 0.f : csq, -st[0], aq);
    float t[16];
#pragma unroll
    for (int i = 0; i < 16; ++i) t[i] = aq[i] * st[1];
    phi16(t);
    const v4u f0 = {pk2(t[0], t[1]), pk2(t[2], t[3]), pk2(t[4], t[5]), pk2(t[6], t[7])};
    const v4u f1 = {pk2(t[8], t[9]), pk2(t[10], t[11]), pk2(t[12], t[13]), pk2(t[14], t[15])};
    SB();
    WAITV(0);
    if (arrive_last(lds, C_CNT + 8, 8, lane)) { if (lane < 8) st_flag(ws, WS_FLAGA + lane * 8192 + b * 32, tag); }
    MEMB();
    TS();
    const int so = WS_PQ + (m32 * 32 + 4 * mem + 2 * kh) * 1024;
    ST128(f0, rw, lane * 16, so);
    ST128(f1, rw, lane * 16, so + 1024);
    WAITV(0);
    if (arrive_last(lds, C_CNT, 4, lane)) { if (lane == 0) st_flag(ws, WS_FLAGQ + panel * 128 + mem * 8, tag); }
    MEMB();
#pragma unroll
    for (int q = 0; q < 4; ++q)
      xres[q] = __builtin_amdgcn_raw_buffer_load_b64(rx, r32 * 1024 + h2 * 8 + q * 16, (panel * 64 + mg * 32) * 1024 + (mem * 64 + kh * 32) * 2, 0);
    TS();
  }

  const LB ta0 = mkbase(ldsb + (4 * h2 + q4) * 128 + ((q4 >> 1) ^ kh) * 64 + trcol + kh * 6144);
  const LB ta1 = mkbase(ldsb + (4 * h2 + q4) * 128 + ((q4 >> 1) ^ kh ^ 1) * 64 + trcol + kh * 6144);
  v4u hres[2];
  {
    wait1(lds, C_GO + 4, 1);
    v4u pq[16];
#pragma unroll
    for (int i = 0; i < 16; ++i) pq[i] = LD128(rw, lane * 16, WS_PQ + (m32 * 32 + 16 * kh + i) * 1024);
    TS();
    wait4(lds, C_LAND, LB0 + 6);
    v4u wf[3][2];
#pragma unroll
    for (int i = 0; i < 2; ++i) { wf[i][0] = tr_frag(ta0, slot_of(0) + i * 2048, 1024); wf[i][1] = tr_frag(ta1, slot_of(0) + i * 2048, 1024); }
    wait1(lds, C_GO, 1);
    TS();
    {
      v4u kl[4][2], zl[2];
      const int kvv = h2 * 1024 + r32 * 16;
#pragma unroll
      for (int i = 0; i < 4; ++i) {
        kl[i][0] = LD128(rw, kvv, WS_KV + (8 * w + 2 * i) * 1024);
        kl[i][1] = LD128(rw, kvv, WS_KV + (8 * w + 2 * i) * 1024 + 512);
      }
      const int zv = lane < 16 ? (lane >> 1) * 64 + (lane & 1) * 16 : OOB;
      zl[0] = LD128(rw, zv, WS_Z + w * 512);
      zl[1] = LD128(rw, zv, WS_Z + w * 512 + 32);
      auto cv = [&](v4u x, v4u y) LAM {
        return v4u{pk2(asf(x[0]), asf(x[1])), pk2(asf(x[2]), asf(x[3])), pk2(asf(y[0]), asf(y[1])), pk2(asf(y[2]), asf(y[3]))};
      };
#pragma unroll
      for (int i = 0; i < 4; ++i) *(LAS v4u*)(lds + L_X + (8 * w + 2 * i) * 512 + lane * 16) = cv(kl[i][0], kl[i][1]);
      if (lane < 16) *(LAS v4u*)(lds + L_KZ + w * 256 + lane * 16) = cv(zl[0], zl[1]);
    }
    post(lds, mymp, 12);
    wait1(lds, C_MP + (w ^ 1) * 4, 12);
    v4u kf[16];
    {
      const int kb = (r32 < 16) ? L_X + kh * 8192 + (h2 * 16 + r32) * 16 : L_KZ + kh * 512 + h2 * 16;
      const int ks = (r32 < 16) ? 512 : 32;
#pragma unroll
      for (int i = 0; i < 16; ++i) kf[i] = *(const LAS v4u*)(lds + kb + i * ks);
    }
    post(lds, mymp, 13);
    TS();
    f32x16 acc[2] = {};
    f32x16 o[2];
    v4u of[2];
    const f32x16 zero = {};
    auto scale_lo = [&](const f32x16& oc, float& inv, v4u& dst) LAM {
      inv = __builtin_amdgcn_rcpf(oc[8] + 1e-6f);
      dst[0] = pk2(oc[0] * inv, oc[1] * inv); dst[1] = pk2(oc[2] * inv, oc[3] * inv);
    };
    auto scale_hi = [&](const f32x16& oc, float inv, v4u& dst) LAM {
      dst[2] = pk2(oc[4] * inv, oc[5] * inv); dst[3] = pk2(oc[6] * inv, oc[7] * inv);
    };
    float inv;
    o[0] = MFMA32(kf[0], pq[0], zero);
    o[1] = MFMA32(kf[1], pq[1], zero);
    SB();
    scale_lo(o[0], inv, of[0]); scale_hi(o[0], inv, of[0]);
    SB();
#pragma unroll
    for (int hd = 0; hd < 16; ++hd) {
      acc[0] = MFMA32(wf[hd % 3][0], of[hd & 1], acc[0]);
      if (hd + 1 < 16) scale_lo(o[(hd + 1) & 1], inv, of[(hd + 1) & 1]);
      SB();
      acc[1] = MFMA32(wf[hd % 3][1], of[hd & 1], acc[1]);
      if (hd + 1 < 16) scale_hi(o[(hd + 1) & 1], inv, of[(hd + 1) & 1]);
      SB();
      if (hd + 2 < 16) {
        o[hd & 1] = MFMA32(kf[hd + 2], pq[hd + 2], zero);
        const int sl = slot_of((hd + 2) / 3) + ((hd + 2) % 3) * 2048;
        wf[(hd + 2) % 3][0] = tr_frag(ta0, sl, 1024);
        wf[(hd + 2) % 3][1] = tr_frag(ta1, sl, 1024);
      }
      SB();
    }
    TS();
    wait1(lds, C_MP + (w ^ 1) * 4, 13);
#pragma unroll
    for (int q = 0; q < 4; ++q) xch[(w * 4 + q) * 64 + lane] = f32x4{acc[1][4 * q], acc[1][4 * q + 1], acc[1][4 * q + 2], acc[1][4 * q + 3]};
    post(lds, mymp, 14);
    wait1(lds, C_MP + (w ^ 2) * 4, 14);
    float s1 = 0.f, s2 = 0.f;
#pragma unroll
    for (int q = 0; q < 4; ++q) {
      const f32x4 t = xch[((w ^ 2) * 4 + q) * 64 + lane];
      const unsigned ua = pk2(acc[0][4 * q] + t[0] + bflo(xres[q].x), acc[0][4 * q + 1] + t[1] + bfhi(xres[q].x));
      const unsigned ub = pk2(acc[0][4 * q + 2] + t[2] + bflo(xres[q].y), acc[0][4 * q + 3] + t[3] + bfhi(xres[q].y));
      hres[q >> 1][(q & 1) * 2] = ua;
      hres[q >> 1][(q & 1) * 2 + 1] = ub;
    }
    const int so = WS_H + (m32 * 32 + 4 * mem + 2 * kh) * 1024;
    post(lds, mymp, 15);
    hres[0] = clean4(hres[0]); hres[1] = clean4(hres[1]);
    ST128(hres[0], rw, lane * 16, so);
    ST128(hres[1], rw, lane * 16, so + 1024);
    SB();
#pragma unroll
    for (int q = 0; q < 8; ++q) {
      const unsigned u = hres[q >> 2][q & 3];
      const float e0 = bflo(u), e1 = bfhi(u);
      s1 += e0 + e1;
      s2 += e0 * e0 + e1 * e1;
    }
    s1 = half_sum(s1); s2 = half_sum(s2);
    const v2u st = {clean(asu(s1)), clean(asu(s2))};
    ST64(st, rw, lane < 32 ? lane * 8 : OOB, WS_HS + m32 * 4096 + (mem * 2 + kh) * 256);
    TS();
  }

  {
    const LB tc = mkbase(ldsb + (4 * h2 + q4) * 64 + trcol);
    const int c32 = pp * 4 + w;
    v4u R[2][6];
    wait4(lds, C_LAND, LB0 + 14);
#pragma unroll
    for (int s = 0; s < 6; ++s) R[0][s] = tr_frag(tc, slot_of(6) + (s % 3) * 4096 + (s / 3) * 1024, 512);
    v4u hf[3][4];
    const int hso = WS_H + c32 * 32 * 1024;
    auto ldH = [&](int g) LAM {
#pragma unroll
      for (int i = 0; i < 4; ++i) hf[g % 3][i] = LD128(rw, lane * 16, hso + (4 * g + i) * 1024);
    };
    auto okH = [&](int g) LAM {
      return all_lanes(umax(umax(max4(hf[g % 3][0]), max4(hf[g % 3][1])), umax(max4(hf[g % 3][2]), max4(hf[g % 3][3]))) != POISON);
    };
    __builtin_amdgcn_s_sleep(FIRSTSLEEP);
    ldH(0); ldH(1); ldH(2);
    SB();
    f32x16 acc[3] = {};
#pragma unroll
    for (int j = 0; j < 16; ++j) {
      if ((j & 1) == 0) {
        const int g = j >> 1;
        if (!okH(g)) {
#pragma nounroll
          for (unsigned sp = 0; sp < SPIN_CAP; ++sp) {
            MEMB();
            __builtin_amdgcn_s_sleep(PSLEEP);
            ldH(g);
            if (okH(g)) break;
          }
          if (g + 1 < 8) ldH(g + 1);
          if (g + 2 < 8) ldH(g + 2);
        }
        SB();
        if (j == 0) { post(lds, mymp, 20); TS(); }
      }
#pragma unroll
      for (int s = 0; s < 6; ++s) {
        const int ks = 2 * j + s / 3, tt = s % 3;
        if (j + 1 < 16) R[(j + 1) & 1][s] = tr_frag(tc, slot_of(6 + ((j + 1) >> 1)) + tt * 4096 + (((j + 1) & 1) * 2 + s / 3) * 1024, 512);
        acc[tt] = MFMA32(R[j & 1][s], hf[(ks >> 2) % 3][ks & 3], acc[tt]);
        if (tt == 2 && ks + 12 < 32) hf[(ks >> 2) % 3][ks & 3] = LD128(rw, lane * 16, hso + (ks + 12) * 1024);
        SB();
      }
      if ((j & 3) == 2) post(lds, mymp, 20 + (j >> 1));
    }
    TS();
    wait4(lds, C_MP, 15);
#pragma unroll
    for (int t = 0; t < 3; ++t) {
      xch[(w * 6 + 2 * t) * 64 + lane] = f32x4{acc[t][8], acc[t][9], acc[t][10], acc[t][11]};
      xch[(w * 6 + 2 * t + 1) * 64 + lane] = f32x4{acc[t][12], acc[t][13], acc[t][14], acc[t][15]};
    }
    post(lds, mymp, 40);
    wait4(lds, C_HP, 2);
    {
      const f32x2 st = *(const LAS f32x2*)(lds + L_ST2 + (w * 32 + r32) * 8);
      float o[24];
#pragma unroll
      for (int t = 0; t < 3; ++t)
#pragma unroll
        for (int qq = 0; qq < 2; ++qq) {
          const f32x4 cs = *(const LAS f32x4*)(lds + L_CS1 + (t * 32 + 8 * qq + 4 * h2) * 4);
          const f32x4 bb = *(const LAS f32x4*)(lds + L_B1 + (t * 32 + 8 * qq + 4 * h2) * 4);
#pragma unroll
          for (int i = 0; i < 4; ++i) o[8 * t + 4 * qq + i] = acc[t][4 * qq + i] * st[1] + (st[0] * cs[i] + bb[i]);
        }
#pragma unroll
      for (int t = 0; t < 3; ++t) {
        mishN<8>(o + 8 * t);
        const v4u af = {pk2(o[8 * t], o[8 * t + 1]), pk2(o[8 * t + 2], o[8 * t + 3]), pk2(o[8 * t + 4], o[8 * t + 5]), pk2(o[8 * t + 6], o[8 * t + 7])};
        ST128(clean4(af), rw, (t < ntile) ? lane * 16 : OOB, WS_ACT + (c32 * 86 + 2 * (tile0 + t)) * 1024);
        SB();
      }
    }
    TS();
  }

  {
    v4u R[2][6];
    unsigned landed = wait4(lds, C_LAND, LB0 + 16);
#pragma unroll
    for (int s = 0; s < 3; ++s) { R[0][s] = tr_frag(ta0, slot_of(14) + s * 2048, 1024); R[0][3 + s] = tr_frag(ta1, slot_of(14) + s * 2048, 1024); }
    constexpr int NG = 5;
    v4u af[NG][3];
    const int aso = WS_ACT + (m32 * 86 + 43 * kh) * 1024;
    auto ldA = [&](int g) LAM {
#pragma unroll
      for (int i = 0; i < 3; ++i) if (3 * g + i < 43) af[g % NG][i] = LD128(rw, lane * 16, aso + (3 * g + i) * 1024);
    };
    auto okA = [&](int g) LAM {
      unsigned m = max4(af[g % NG][0]);
      if (g < 14) m = umax(m, umax(max4(af[g % NG][1]), max4(af[g % NG][2])));
      return all_lanes(m != POISON);
    };
    __builtin_amdgcn_s_sleep(FIRSTSLEEP);
#pragma unroll
    for (int g = 0; g < NG; ++g) ldA(g);
    SB();
    f32x16 acc[2] = {};
#pragma unroll
    for (int c = 0; c < 15; ++c) {
      if (c + 1 < 15 && landed < (unsigned)(LB0 + 14 + c + 2)) landed = wait4(lds, C_LAND, LB0 + 14 + c + 2);
      if (!okA(c)) {
#pragma nounroll
        for (unsigned sp = 0; sp < SPIN_CAP; ++sp) {
          MEMB();
          __builtin_amdgcn_s_sleep(PSLEEP);
          ldA(c);
          if (okA(c)) break;
        }
#pragma unroll
        for (int g = c + 1; g < c + NG; ++g) if (g < 15) ldA(g);
      }
      SB();
      if (c == 0) TS();
#pragma unroll
      for (int s = 0; s < 3; ++s) {
        const int ks = c * 3 + s;
        if (ks + 3 < 43) {
          R[(c + 1) & 1][s] = tr_frag(ta0, slot_of(14 + c + 1) + s * 2048, 1024);
          R[(c + 1) & 1][3 + s] = tr_frag(ta1, slot_of(14 + c + 1) + s * 2048, 1024);
        }
        if (ks < 43) {
          const v4u a = af[c % NG][s];
          acc[0] = MFMA32(R[c & 1][s], a, acc[0]);
          acc[1] = MFMA32(R[c & 1][3 + s], a, acc[1]);
          if (ks + 3 * NG < 43) af[c % NG][s] = LD128(rw, lane * 16, aso + (ks + 3 * NG) * 1024);
        }
        SB();
      }
      if (c == 3) post(lds, mymp, 54);
    }
    TS();
    wait4(lds, C_HP, 3);
#pragma unroll
    for (int q = 0; q < 4; ++q) xch[(w * 4 + q) * 64 + lane] = f32x4{acc[1][4 * q], acc[1][4 * q + 1], acc[1][4 * q + 2], acc[1][4 * q + 3]};
    post(lds, mymp, 70);
    wait1(lds, C_MP + (w ^ 2) * 4, 70);
    int lz = tid; asm volatile("" : "+v"(lz));
    const rsrc_t rout = __builtin_amdgcn_make_buffer_rsrc((void*)p.out, 0, S_ * D_ * 2, 0x00020000);
    const int oo = (lz & 31) * 1024 + ((lz >> 5) & 1) * 8;
    const int so = (panel * 64 + mg * 32) * 1024 + (mem * 64 + kh * 32) * 2;
#pragma unroll
    for (int q = 0; q < 4; ++q) {
      const f32x4 t = xch[((w ^ 2) * 4 + q) * 64 + lane];
      const unsigned ha = hres[q >> 1][(q & 1) * 2], hb = hres[q >> 1][(q & 1) * 2 + 1];
      v2u o;
      o.x = pk2(acc[0][4 * q] + t[0] + bflo(ha), acc[0][4 * q + 1] + t[1] + bfhi(ha));
      o.y = pk2(acc[0][4 * q + 2] + t[2] + bflo(hb), acc[0][4 * q + 3] + t[3] + bfhi(hb));
      __builtin_amdgcn_raw_buffer_store_b64(o, rout, oo + q * 16, so, 0);
    }
    TS();
    TSDUMP();
  }
}

__global__ void k_last(char* ws) { const int lane = threadIdx.x;

    {
      u64 m = ~0ull;
      for (int i = 0; i < 4; ++i) { const u64 v = ld_flag(ws, WS_T0 + (lane + 64 * i) * 8); m = v < m ? v : m; }
      for (int o = 32; o > 0; o >>= 1) { const u64 v = __shfl_xor(m, o); m = v < m ? v : m; }
      const u64 t1 = __builtin_amdgcn_s_memrealtime();
      if (t1 - m < 5000ull) while (__builtin_amdgcn_s_memrealtime() - m < 5000ull) __builtin_amdgcn_s_sleep(1);
    }
}

extern "C" void kernel_launch(void* const* d_in, const int* in_sizes, int n_in, void* d_out, int out_size, void* d_ws, size_t ws_size,
                              hipStream_t stream) {
  Params p;
  p.x = (const char*)d_in[0]; p.wqkv = (const char*)d_in[1]; p.wo = (const char*)d_in[2];
  p.w1 = (const char*)d_in[4]; p.b1 = (const char*)d_in[5]; p.w2 = (const char*)d_in[6];
  p.out = (char*)d_out; p.ws = (char*)d_ws;
  (void)hipFuncSetAttribute((const void*)fused_block, hipFuncAttributeMaxDynamicSharedMemorySize, LDS_BYTES);
  fused_block<<<256, 512, LDS_BYTES, stream>>>(p);
  k_last<<<1, 64, 0, stream>>>(p.ws);
}
```

```cpp
#include <hip/hip_runtime.h>
#include <hip/hip_bf16.h>
#include <stdint.h>

#if defined(__HIP_DEVICE_COMPILE__)
#define NOPK __attribute__((target("no-packed-fp32-ops")))
#else
#define NOPK
#endif
#define DI __device__ __forceinline__ NOPK
#define LAM NOPK __attribute__((always_inline))
#define SB() __builtin_amdgcn_sched_barrier(0)
#define MEMB() asm volatile("" ::: "memory")
#define LAS __attribute__((address_space(3)))
#define GAS __attribute__((address_space(1)))
typedef unsigned v4u __attribute__((ext_vector_type(4)));
typedef unsigned v2u __attribute__((ext_vector_type(2)));
typedef short s16x4 __attribute__((ext_vector_type(4)));
typedef __bf16 bf16x8 __attribute__((ext_vector_type(8)));
typedef __bf16 bf16x2 __attribute__((ext_vector_type(2)));
typedef float f32x16 __attribute__((ext_vector_type(16)));
typedef float f32x4 __attribute__((ext_vector_type(4)));
typedef float f32x2 __attribute__((ext_vector_type(2)));
typedef unsigned long long u64;
typedef __amdgpu_buffer_rsrc_t rsrc_t;

constexpr int S_ = 2048, D_ = 512, F_ = 1376;
constexpr int WS_EPOCH = 0;
constexpr int WS_FLAGP = 4096;
constexpr int WS_XST = 32768;
constexpr int WS_CSQ = 65536;
constexpr int WS_CS1 = 163840;
constexpr int WS_S1Z = 262144;
constexpr int WS_KV = 327680;
constexpr int KVREP = 36864, KVZ = 32768;
constexpr int WS_HS = 655360;
constexpr int WS_PQ = 1 << 20;
constexpr int WS_H = 3 << 20;
constexpr int WS_ACT = 5 << 20;
constexpr int WS_S1 = WS_ACT + 64 * 86 * 1024;
constexpr int WS_BYTES = WS_S1 + (1 << 20);
constexpr int CODE_KB = 45;
constexpr int WS_STAMP = 240 << 20;
constexpr int RSTRIDE = 12 << 20;
constexpr int OOB = 0x40000000;

constexpr int L_ST = 147456;
constexpr int L_CS = 147968;
constexpr int L_RED = 148992;
constexpr int SLOT = 12288, NSLOT = 10;
constexpr int L_X = 122880;
constexpr int L_KZ = L_X + 16384;
constexpr int L_C1 = L_X + 20480;
constexpr int L_ST2 = 159232;
constexpr int L_CS1 = 160256;
constexpr int L_B1 = 161024;
constexpr int L_WARM = 161792;
constexpr int L_CTL = 163584;
constexpr int C_LAND = L_CTL, C_MP = L_CTL + 16, C_HP = L_CTL + 32, C_GO = L_CTL + 48, C_CNT = L_CTL + 64, C_LAND2 = L_CTL + 96, C_TAG = L_CTL + 128;
constexpr int LDS_BYTES = 163840;
#define slot_of(c) (((c) % NSLOT) * SLOT)
#define wa_of(c) (65536 + ((c) % 5) * 16384)
constexpr int LB0 = 12;

constexpr int NSTG = 14;

#define ROT_A 2
#define CODEPF 1
#define ROT_C 0
#define ROT_D 0
#define W2S 0
#define NEARLY 4
#define KVORD 2
#define POIS 2
#define QSLEEP 8
#define NREG 16
#define GQ0 8
#define GQ1 15
#define WO3 1
#define RDLY 10
#define STG1 0
#define W69 0
#define RSLEEP 1
#define TOUCH 1
#define OUTAUX 0
#define STG 1
#define FIRSTSLEEP 8
#define MAP42 1
#define PSLEEP 2
#define LSLEEP 1
constexpr int AUX_SC1 = 16;
constexpr unsigned SPIN_CAP = 1u << 16;
constexpr unsigned LSPIN_CAP = 1u << 22;

#define MFMA32(a, b, c) __builtin_amdgcn_mfma_f32_32x32x16_bf16(__builtin_bit_cast(bf16x8, a), __builtin_bit_cast(bf16x8, b), c, 0, 0, 0)
#define WAITV_(n) asm volatile("s_waitcnt vmcnt(" #n ")" ::: "memory")
#define WAITV(n) WAITV_(n)
#define RLX_AGENT __ATOMIC_RELAXED, __HIP_MEMORY_SCOPE_AGENT
#define LD128(rs, vo, so) __builtin_amdgcn_raw_buffer_load_b128(rs, vo, so, AUX_SC1)
#define LD64(rs, vo, so) __builtin_amdgcn_raw_buffer_load_b64(rs, vo, so, AUX_SC1)
#define ST128(v, rs, vo, so) __builtin_amdgcn_raw_buffer_store_b128(v, rs, (vo) + (so), 0, AUX_SC1)
#define ST64(v, rs, vo, so) __builtin_amdgcn_raw_buffer_store_b64(v, rs, vo, so, AUX_SC1)

DI float asf(unsigned u) { return __builtin_bit_cast(float, u); }
DI unsigned asu(float f) { return __builtin_bit_cast(unsigned, f); }
DI unsigned pk2(float a, float b) {
  f32x2 f = {a, b};
  return __builtin_bit_cast(unsigned, __builtin_convertvector(f, bf16x2));
}
DI float bflo(unsigned u) { return asf(u << 16); }
DI float bfhi(unsigned u) { return asf(u & 0xffff0000u); }
struct LB { unsigned a[3]; };
DI LB mkbase(unsigned a) {
  LB r; r.a[0] = a; r.a[1] = a + 65536u; r.a[2] = a + 131072u;
  asm volatile("" : "+v"(r.a[0]), "+v"(r.a[1]), "+v"(r.a[2]));
  return r;
}
DI const LAS char* lp(const LB& b, int off) { return (const LAS char*)(size_t)b.a[off >> 16] + (off & 65535); }
DI v4u tr_frag(const LB& b, int off, int dt) {
  s16x4 x = __builtin_amdgcn_ds_read_tr16_b64_v4i16((LAS s16x4*)lp(b, off));
  s16x4 y = __builtin_amdgcn_ds_read_tr16_b64_v4i16((LAS s16x4*)lp(b, off + dt));
  v2u ux = __builtin_bit_cast(v2u, x), uy = __builtin_bit_cast(v2u, y);
  return v4u{ux.x, ux.y, uy.x, uy.y};
}
DI float half_sum(float v) {
  auto r = __builtin_amdgcn_permlane32_swap(asu(v), asu(v), false, false);
  const unsigned r0 = r[0], r1 = r[1];
  return asf(r0) + asf(r1);
}
DI float row_pair_sum(float v) {
  auto r = __builtin_amdgcn_permlane16_swap(asu(v), asu(v), false, false);
  const unsigned r0 = r[0], r1 = r[1];
  return asf(r0) + asf(r1);
}
template <int CTRL> DI float dpp_add(float v) { return v + asf(__builtin_amdgcn_update_dpp(0, asu(v), CTRL, 0xf, 0xf, true)); }
DI float wave_sum(float v) {
  v = dpp_add<0xB1>(v); v = dpp_add<0x4E>(v); v = dpp_add<0x141>(v); v = dpp_add<0x140>(v);
  return half_sum(row_pair_sum(v));
}
constexpr unsigned POISON = 0xffffffffu;
DI unsigned umax(unsigned a, unsigned b) { return a > b ? a : b; }
DI unsigned clean(unsigned d) { return d < 0xfffffffeu ? d : 0xfffffffeu; }
DI v4u clean4(v4u v) { return v4u{clean(v.x), clean(v.y), clean(v.z), clean(v.w)}; }
DI unsigned max4(v4u v) { return umax(umax(v.x, v.y), umax(v.z, v.w)); }
DI bool all_lanes(bool ok) { return __builtin_amdgcn_ballot_w64(!ok) == 0ull; }
DI unsigned tagA(unsigned tlo) { return tlo * 0x9E3779B1u; }
DI unsigned tagB(unsigned tlo) { return tlo * 0x85EBCA77u; }
DI v4u tag24(f32x4 v, unsigned t) {
  v4u r;
#pragma unroll
  for (int i = 0; i < 4; ++i) r[i] = ((asu(v[i]) + 0x80u) & 0xffffff00u) | ((t >> (8 * i)) & 0xffu);
  return r;
}
DI unsigned bad24(v4u g, unsigned t) { return ((g.x ^ t) | (g.y ^ (t >> 8)) | (g.z ^ (t >> 16)) | (g.w ^ (t >> 24))) & 0xffu; }
DI f32x4 val24(v4u g) { return f32x4{asf(g.x & 0xffffff00u), asf(g.y & 0xffffff00u), asf(g.z & 0xffffff00u), asf(g.w & 0xffffff00u)}; }
DI v4u tag16(f32x4 v, unsigned ta, unsigned tb) {
  const unsigned p0 = pk2(v[0], v[1]), p1 = pk2(v[2], v[3]);
  return v4u{(p0 << 16) | (ta & 0xffffu), (p0 & 0xffff0000u) | (ta >> 16), (p1 << 16) | (tb & 0xffffu), (p1 & 0xffff0000u) | (tb >> 16)};
}
DI unsigned lo16s(unsigned a, unsigned b) { return __builtin_amdgcn_perm(b, a, 0x05040100u); }
DI unsigned hi16s(unsigned a, unsigned b) { return __builtin_amdgcn_perm(b, a, 0x07060302u); }
DI unsigned bad16(v4u g, unsigned ta, unsigned tb) { return (lo16s(g.x, g.y) ^ ta) | (lo16s(g.z, g.w) ^ tb); }
DI f32x16 rank1(float a, float b, f32x16 c) {
  asm volatile("s_nop 7\n\ts_nop 7" : "+v"(c));
  c = __builtin_amdgcn_mfma_f32_32x32x2f32(a, b, c, 0, 0, 0);
  return c;
}
constexpr float LOG2E = 1.44269504088896341f;
DI void phi16(float (&t)[16]) {
  float e[16];
#pragma unroll
  for (int i = 0; i < 16; ++i) e[i] = __builtin_fminf(t[i], 0.f) * LOG2E;
  SB();
#pragma unroll
  for (int i = 0; i < 16; ++i) { e[i] = __builtin_amdgcn_exp2f(e[i]); t[i] = __builtin_fmaxf(t[i], 0.f) + 1e-6f; }
  SB();
#pragma unroll
  for (int i = 0; i < 16; ++i) t[i] += e[i];
}
constexpr float LN2 = 0.693147180559945309f;
template <int N> DI void mishN(float* a) {
  float u[N];
#pragma unroll
  for (int i = 0; i < N; ++i) u[i] = __builtin_amdgcn_exp2f(a[i]) + 1.f;
#pragma unroll
  for (int i = 0; i < N; ++i) u[i] = __builtin_amdgcn_rcpf(u[i] * u[i] + 1.f);
#pragma unroll
  for (int i = 0; i < N; ++i) a[i] = a[i] * (LN2 - (2.f * LN2) * u[i]);
}
DI void glds(rsrc_t rs, int ldsoff, int voff, int soff) {
  asm volatile("s_mov_b32 m0, %0\n\ts_nop 3\n\tbuffer_load_dwordx4 %1, %2, %3 offen lds" ::"s"(ldsoff), "v"(voff), "s"(rs), "s"(soff) : "memory");
}
DI u64 ld_flag(const char* ws, int off) { return __hip_atomic_load((const GAS u64*)(ws + off), RLX_AGENT); }
DI void st_flag(char* ws, int off, u64 v) { __hip_atomic_store((GAS u64*)(ws + off), v, RLX_AGENT); }
DI void wait_flags(const char* ws, int off, u64 tag) {
#pragma nounroll
  for (unsigned sp = 0; sp < SPIN_CAP; ++sp) {
    const bool ok = ld_flag(ws, off) == tag;
    if (all_lanes(ok)) break;
    __builtin_amdgcn_s_sleep(PSLEEP);
  }
  __builtin_amdgcn_fence(__ATOMIC_ACQUIRE, "wavefront");
}
DI unsigned min4(v4u v) {
  const unsigned a = v.x < v.y ? v.x : v.y, b = v.z < v.w ? v.z : v.w;
  return __builtin_amdgcn_readfirstlane(a < b ? a : b);
}
DI unsigned wait4(LAS char* lds, int off, unsigned e) {
  unsigned m = 0;
  SB();
#pragma nounroll
  for (unsigned sp = 0; sp < LSPIN_CAP; ++sp) {
    m = min4(*(volatile LAS v4u*)(lds + off));
    if (m >= e) break;
    __builtin_amdgcn_s_sleep(LSLEEP);
  }
  MEMB(); SB();
  return m;
}
DI void wait1(LAS char* lds, int off, unsigned e) {
  SB();
#pragma nounroll
  for (unsigned sp = 0; sp < LSPIN_CAP; ++sp) {
    if (__builtin_amdgcn_readfirstlane(*(volatile LAS unsigned*)(lds + off)) >= e) break;
    __builtin_amdgcn_s_sleep(LSLEEP);
  }
  MEMB(); SB();
}
DI void post(LAS char* lds, int off, unsigned v) {
  SB(); MEMB();
  *(volatile LAS unsigned*)(lds + off) = v;
  MEMB(); SB();
}
DI bool arrive_last(LAS char* lds, int off, unsigned n, int lane) {
  unsigned old = 0;
  if (lane == 0) old = __hip_atomic_fetch_add((LAS unsigned*)(lds + off), 1u, __ATOMIC_RELAXED, __HIP_MEMORY_SCOPE_WORKGROUP);
  return __builtin_amdgcn_readfirstlane(old) == n - 1;
}

#define TS()
#define TSDUMP()

struct Params {
  const char* x; const char* wqkv; const char* wo; const char* w1; const char* b1; const char* w2;
  char* out; u64 wsr;
};

__global__ __launch_bounds__(512, 2) NOPK void fused_block(Params p) {
  extern __shared__ __attribute__((aligned(16))) char smem[];
  LAS char* lds = (LAS char*)smem;
  const int tid = threadIdx.x, lane = tid & 63;
  const int w = __builtin_amdgcn_readfirstlane(tid >> 6);
  const int b = blockIdx.x, xcd = b & 7, loc = b >> 3;
  if (tid < 32) *(LAS unsigned*)(lds + L_CTL + tid * 4) = 0u;
  const u64 pc0 = __builtin_amdgcn_s_getpc() & ~255ull;
  auto codepf = [&](int piece) LAM {
    const rsrc_t rc = __builtin_amdgcn_make_buffer_rsrc((void*)pc0, 0, CODE_KB * 1024, 0x00020000);
    glds(rc, L_WARM, lane * 16, piece * 1024);
  };
  codepf((loc & 7) * 8 + w);
  const int panel = (xcd >> 1) * 8 + (loc >> 2), mem = (xcd & 1) * 4 + (loc & 3);
  const int pp = panel >> 1, cm = (panel & 1) * 8 + mem;
  const int ntile = cm < 11 ? 3 : 2, tile0 = min(3 * cm, 2 * cm + 11);
  char* ws = (char*)(p.wsr & ~255ull);
  const int h2 = lane >> 5, r32 = lane & 31;
  const int w4 = w & 3;
  TS();
  const int mg = w & 1, kh = w4 >> 1;
  const int m32 = panel * 2 + mg;
  auto mkrw = [&](unsigned tl) LAM { return __builtin_amdgcn_make_buffer_rsrc((void*)(ws + (size_t)(tl & (unsigned)p.wsr & 255u) * RSTRIDE), 0, WS_BYTES, 0x00020000); };
  const rsrc_t rx = __builtin_amdgcn_make_buffer_rsrc((void*)p.x, 0, S_ * D_ * 2, 0x00020000);
  const rsrc_t rq = __builtin_amdgcn_make_buffer_rsrc((void*)p.wqkv, 0, D_ * 3 * D_ * 2, 0x00020000);
  const rsrc_t r1 = __builtin_amdgcn_make_buffer_rsrc((void*)p.w1, 0, D_ * F_ * 2, 0x00020000);
  const unsigned ldsb = (unsigned)(size_t)lds;
  const int q4 = (lane & 15) >> 2;
  const int trcol = ((lane >> 4) & 1) * 32 + (lane & 3) * 8;
  const int xrow = mg * 32 + r32;
  const int xsw = (xrow >> 1) & 7;
  LAS f32x4* xch = (LAS f32x4*)(lds + L_X);
  const int rot = ROT_A == 1 ? (mem + panel) & 7 : ROT_A == 2 ? (2 * mem + panel) & 7 : 0;
  const int rotc = ROT_C ? 2 * (mem & 3) + (panel & 1) : 0, rotd = ROT_D ? 11 * (mem & 3) : 0;
  auto kgC = [&](int g) LAM { return (g + rotc) & 7; };
  auto ksD = [&](int k) LAM { const int t = k + rotd; return t >= 43 ? t - 43 : t; };

  const int lw = w4;
    const rsrc_t ro = __builtin_amdgcn_make_buffer_rsrc((void*)p.wo, 0, D_ * D_ * 2, 0x00020000);
    const rsrc_t r2 = __builtin_amdgcn_make_buffer_rsrc((void*)p.w2, 0, F_ * D_ * 2, 0x00020000);
    const int wsw = ((lane & 7) ^ (((lane >> 4) & 1) << 2)) * 16;
    const int vo = (lane >> 3) * 1024 + wsw;
    const int v1 = (lane >> 2) * 2752 + (lane & 3) * 16;
    const int v1x = ntile == 3 ? v1 : OOB;
    auto pieceW = [&](int c, int u) LAM {
      const int sl = slot_of(c);
      if (c < 6) {
        const int q = lw + 4 * u, k2 = q >= 6 ? 1 : 0, i8 = q - 6 * k2;
        glds(ro, sl + q * 1024, (c == 5 && i8 >= 2) ? OOB : vo, (k2 * 256 + 48 * c + 8 * i8) * 1024 + mem * 128);
      } else if (c < 14) {
        glds(r1, sl + u * 4096 + lw * 1024, u < 2 ? v1 : v1x, (64 * kgC(c - 6) + 16 * lw) * 2752 + (tile0 + u) * 64);
      }
    };
    auto issueW = [&](int c) LAM {
#pragma unroll
      for (int u = 0; u < 3; ++u) pieceW(c, u);
    };
  const int myland = C_LAND + lw * 4;

    auto mkvx = [&](int l) LAM { return (l >> 3) * 1024 + (((l & 7) ^ (((lw & 1) * 4 + (l >> 4)) & 7)) * 16); };
    auto mkvq = [&](int l) LAM { return (l >> 3) * 3072 + ((l & 7) ^ (((l >> 4) & 1) << 2)) * 16; };
    auto issueA = [&](int c, int vx, int vq) LAM {
      if (c < 8) {
        const int kc = (c + rot) & 7;
#pragma unroll
        for (int j = 0; j < 2; ++j) { const int i = lw + 4 * j; glds(rx, c * 8192 + i * 1024, vx, (panel * 64 + 8 * i) * 1024 + kc * 128); }
#pragma unroll
        for (int j = 0; j < 2; ++j)
#pragma unroll
          for (int t = 0; t < 2; ++t) {
            const int i = lw + 4 * j;
            glds(rq, wa_of(c) + t * 8192 + i * 1024, vq, (kc * 64 + 8 * i) * 3072 + (t + 1) * 1024 + mem * 128);
          }
      } else {
#pragma unroll
        for (int t = 0; t < 2; ++t) {
          const int kc = (2 * (c - 8) + t + rot) & 7;
#pragma unroll
          for (int j = 0; j < 2; ++j) { const int i = lw + 4 * j; glds(rq, wa_of(c) + t * 8192 + i * 1024, vq, (kc * 64 + 8 * i) * 3072 + mem * 128); }
        }
      }
    };
  const int cb = panel >> 3, rb = panel & 7;
  u64 e;
  v4u xr[2], wr[2];
  const int vx0 = mkvx(lane), vq0 = mkvq(lane);
    unsigned mp = 0;
    v4u mpn;
    auto peek = [&]() LAM { mpn = *(volatile LAS v4u*)(lds + C_MP); };
    auto need = [&](unsigned e) LAM { mp = min4(mpn); if (mp < e) mp = wait4(lds, C_MP, e); };
  TS();
  { const char* px = p.x; asm volatile("" :: "s"(px)); }
  asm volatile("s_waitcnt lgkmcnt(0)\n\ts_barrier" ::: "memory");
  TS();
  if (w >= 4) {
    issueA(0, vx0, vq0);
  } else {
    e = ld_flag(ws, WS_EPOCH) + 1ull;
#pragma unroll
    for (int i = 0; i < 2; ++i) xr[i] = __builtin_amdgcn_raw_buffer_load_b128(rx, lane * 16, (panel * 64 + 8 * mem + 2 * w + i) * 1024, 0);
    const int vq2 = (cb < 3) ? (lane >> 3) * 3072 + (lane & 7) * 16 : OOB;
#pragma unroll
    for (int i = 0; i < 2; ++i) wr[i] = __builtin_amdgcn_raw_buffer_load_b128(rq, vq2, (64 * rb + 16 * w + 8 * i) * 3072 + cb * 1024 + mem * 128, 0);
  }
  SB(); MEMB();
  TS();
  if (__builtin_expect(w >= 4, 1)) {
    issueA(1, vx0, vq0); issueA(2, vx0, vq0);
    TS();
    WAITV(12); post(lds, myland, 1); issueA(3, vx0, vq0);
    TS();
    WAITV(12); post(lds, myland, 2); issueA(4, vx0, vq0);
  }
  int w2 = w;
  asm volatile("" : "+s"(w2));
  if (w2 >= 4) {
    const int myhp = C_HP + lw * 4;
    int l2 = tid; asm volatile("" : "+v"(l2));
    const int vx = mkvx(l2 & 63), vq = mkvq(l2 & 63);
    v4u stg[NSTG][3];
    auto ldW = [&](int c) LAM {
#pragma unroll
      for (int u = 0; u < 3; ++u) {
        if (c < 6) {
          const int q = lw + 4 * u, k2 = q >= 6 ? 1 : 0, i8 = q - 6 * k2;
          stg[4 + c][u] = __builtin_amdgcn_raw_buffer_load_b128(ro, (c == 5 && i8 >= 2) ? OOB : vo, (k2 * 256 + 48 * c + 8 * i8) * 1024 + mem * 128, 0);
        } else if (c < 14) {
          stg[(c - 10) % NSTG][u] = __builtin_amdgcn_raw_buffer_load_b128(r1, u < 2 ? v1 : v1x, (64 * kgC(c - 6) + 16 * lw) * 2752 + (tile0 + u) * 64, 0);
        } else if (3 * (c - 14) + u < 43) {
          stg[(c - 10) % NSTG][u] = __builtin_amdgcn_raw_buffer_load_b128(r2, vo, (kh * 688 + 16 * ksD(3 * (c - 14) + u) + 8 * mg) * 1024 + mem * 128, 0);
        }
      }
      MEMB();
    };
    const LB pw1 = mkbase(ldsb + lw * 1024 + lane * 16);
    const LB pw2 = mkbase(ldsb + kh * 6144 + mg * 1024 + lane * 16);
    auto putW = [&](int c) LAM {
#pragma unroll
      for (int u = 0; u < 3; ++u) {
        if (c < 6) *(LAS v4u*)lp(pw1, slot_of(c) + u * 4096) = stg[4 + c][u];
        else if (c < 14) *(LAS v4u*)lp(pw1, slot_of(c) + u * 4096) = stg[(c - 10) % NSTG][u];
        else if (3 * (c - 14) + u < 43) *(LAS v4u*)lp(pw2, slot_of(c) + u * 2048) = stg[(c - 10) % NSTG][u];
      }
    };
    peek();
    WAITV(12); post(lds, myland, 3); need(1); issueA(5, vx, vq); peek();
    WAITV(12); post(lds, myland, 4); need(2); issueA(6, vx, vq); peek();
    WAITV(12); post(lds, myland, 5); need(3); issueA(7, vx, vq); peek();
    WAITV(12); post(lds, myland, 6); need(4); issueA(8, vx, vq); peek();
    WAITV(10); post(lds, myland, 7); need(5); issueA(9, vx, vq); peek();
    WAITV(8); post(lds, myland, 8);
    TS();
    if (WO3 || lw != 3) { ldW(0); ldW(1); ldW(2); ldW(3); ldW(4); ldW(5); }
    need(7); issueA(10, vx, vq); issueA(11, vx, vq);
    SB(); WAITV(0); post(lds, myland, 12);
    TS();
    u64 tag;
    unsigned tl;
    {
      const v2u tg = *(const LAS v2u*)(lds + C_TAG);
      tl = __builtin_amdgcn_readfirstlane(tg.x);
      const unsigned th = __builtin_amdgcn_readfirstlane(tg.y);
      tag = (u64)tl | ((u64)th << 32);
    }
    const rsrc_t rw = mkrw(tl);
    if (lw == 3) {
      const unsigned ta = tagA(tl), tb = tagB(tl);
      const int so = WS_S1 + (mem * 32 + panel) * 4096, soz = WS_S1Z + (mem * 16 + (panel >> 1)) * 512;
      const bool zr = (panel & 1) == 0;
      v4u g[5];
      wait4(lds, C_MP, RDLY);
#pragma unroll
      for (int i = 0; i < 4; ++i) g[i] = LD128(rw, lane * 16, so + i * 1024);
      g[4] = LD128(rw, zr ? r32 * 16 : OOB, soz);
#pragma nounroll
      for (unsigned sp = 0; sp < SPIN_CAP; ++sp) {
        unsigned bad[5];
#pragma unroll
        for (int i = 0; i < 4; ++i) bad[i] = bad24(g[i], ta);
        bad[4] = zr ? bad24(g[4], ta) : 0u;
        if (all_lanes((((bad[0] | bad[1]) | (bad[2] | bad[3])) | bad[4]) == 0u)) break;
        MEMB();
        __builtin_amdgcn_s_sleep(RSLEEP);
#pragma unroll
        for (int i = 0; i < 4; ++i) if (bad[i]) g[i] = LD128(rw, lane * 16, so + i * 1024);
        if (bad[4]) g[4] = LD128(rw, r32 * 16, soz);
      }
      f32x4 a = (val24(g[0]) + val24(g[1])) + (val24(g[2]) + val24(g[3]));
#pragma unroll
      for (int i = 0; i < 4; ++i) a[i] = half_sum(row_pair_sum(dpp_add<0x128>(a[i])));
      ST128(tag16(a, ta, tb), rw, (lane >> 3) * KVREP + (lane & 7) * 16, WS_KV + mem * 4096 + panel * 128);
      MEMB();
      a = val24(g[4]);
#pragma unroll
      for (int i = 0; i < 4; ++i) a[i] = row_pair_sum(dpp_add<0x140>(dpp_add<0x141>(dpp_add<0x4E>(dpp_add<0xB1>(a[i])))));
      ST128(tag16(a, ta, tb), rw, (zr && lane < 8) ? lane * KVREP : OOB, WS_KV + KVZ + (mem * 16 + (panel >> 1)) * 16);
      MEMB();
      if (!WO3) { ldW(0); ldW(1); ldW(2); ldW(3); ldW(4); ldW(5); }
    } else {
      ldW(10); ldW(11); ldW(12); ldW(13);
    }
    TS();
    wait4(lds, C_MP, 10);
    putW(0); putW(1); putW(2); putW(3); putW(4); putW(5);
    post(lds, myland, LB0 + 6);
    issueW(6); issueW(7); issueW(8); issueW(9);
    TS();
    wait4(lds, C_MP, 12);
    if (b == 0 && lw == 0 && lane == 0) st_flag(ws, WS_EPOCH, tag);
    TS();
    {
      v4u cl[4];
      const int vo1 = ((lane & 15) < 6 && lw * 6 + (lane & 15) < ntile * 8) ? (lane >> 4) * 4 * 5632 + (lane & 15) * 16 : OOB;
#pragma unroll
      for (int i = 0; i < 4; ++i) cl[i] = LD128(rw, vo1, WS_CS1 + i * 5632 + tile0 * 128 + lw * 96);
      const rsrc_t rb = __builtin_amdgcn_make_buffer_rsrc((void*)p.b1, 0, F_ * 2, 0x00020000);
      const v2u bb = __builtin_amdgcn_raw_buffer_load_b64(rb, tile0 * 64 + lw * 48 + lane * 8, 0, 0);
      SB(); WAITV(5);
      post(lds, myland, LB0 + 10);
      const unsigned ta = tagA(tl);
#pragma nounroll
      for (unsigned sp = 0; sp < SPIN_CAP; ++sp) {
        if (all_lanes(vo1 == OOB || ((bad24(cl[0], ta) | bad24(cl[1], ta)) | (bad24(cl[2], ta) | bad24(cl[3], ta))) == 0u)) break;
        MEMB();
        __builtin_amdgcn_s_sleep(PSLEEP);
#pragma unroll
        for (int i = 0; i < 4; ++i) cl[i] = LD128(rw, vo1, WS_CS1 + i * 5632 + tile0 * 128 + lw * 96);
      }
      f32x4 a = (val24(cl[0]) + val24(cl[1])) + (val24(cl[2]) + val24(cl[3]));
#pragma unroll
      for (int i = 0; i < 4; ++i) a[i] = half_sum(row_pair_sum(a[i]));
      if (lane < 6) {
        *(LAS f32x4*)(lds + L_CS1 + lw * 96 + lane * 16) = a;
        *(LAS f32x4*)(lds + L_B1 + lw * 96 + lane * 16) = f32x4{bflo(bb.x) * LOG2E, bfhi(bb.x) * LOG2E, bflo(bb.y) * LOG2E, bfhi(bb.y) * LOG2E};
      }
      post(lds, myhp, 2);
    }
    if (lw == 3 || NEARLY < 4) { ldW(10); ldW(11); ldW(12); ldW(13); }
    ldW(14); ldW(15); ldW(16); ldW(17); ldW(18); ldW(19);
    ldW(20); ldW(21); ldW(22); ldW(23);
    wait4(lds, C_MP, 14);
    putW(10); putW(11); putW(12); putW(13);
    post(lds, myland, LB0 + 14);
    TS();
    ldW(24); ldW(25); ldW(26); ldW(27);
    TS();
    {
      const int c32 = pp * 4 + lw;
      v2u hs[8];
      wait1(lds, C_MP + lw * 4, 20);
#pragma nounroll
      for (unsigned sp = 0; sp < SPIN_CAP; ++sp) {
        unsigned m = 0;
#pragma unroll
        for (int i = 0; i < 8; ++i) hs[i] = LD64(rw, (8 * h2 + i) * 256 + r32 * 8, WS_HS + c32 * 4096);
#pragma unroll
        for (int i = 0; i < 8; ++i) m = umax(m, umax(hs[i].x, hs[i].y));
        if (all_lanes(m != POISON)) break;
        MEMB();
        __builtin_amdgcn_s_sleep(PSLEEP);
      }
      float s1 = 0.f, s2 = 0.f;
#pragma unroll
      for (int i = 0; i < 8; ++i) { s1 += asf(hs[i].x); s2 += asf(hs[i].y); }
      s1 = half_sum(s1); s2 = half_sum(s2);
      const float mu = s1 * (1.f / 512.f);
      const float rstd = __builtin_amdgcn_rsqf(__builtin_fmaxf(s2 * (1.f / 512.f) - mu * mu, 0.f) + 1e-5f) * LOG2E;
      const float nm = -mu * rstd;
      if (lane < 32) *(LAS f32x2*)(lds + L_ST2 + (lw * 32 + r32) * 8) = f32x2{nm, rstd};
      post(lds, myhp, 3);
      wait4(lds, C_HP, 2);
      float o[24];
#pragma unroll
      for (int t = 0; t < 3; ++t)
#pragma unroll
        for (int qq = 0; qq < 2; ++qq) {
          const f32x4 cs = *(const LAS f32x4*)(lds + L_CS1 + (t * 32 + 16 + 8 * qq + 4 * h2) * 4);
          const f32x4 bb = *(const LAS f32x4*)(lds + L_B1 + (t * 32 + 16 + 8 * qq + 4 * h2) * 4);
#pragma unroll
          for (int i = 0; i < 4; ++i) o[8 * t + 4 * qq + i] = nm * cs[i] + bb[i];
        }
#pragma unroll
      for (int i = 0; i < 24; ++i) asm volatile("" : "+v"(o[i]));
      putW(14); putW(15);
      ldW(28);
      wait4(lds, C_MP, 21); putW(16); putW(17);
      wait4(lds, C_MP, 23); putW(18); putW(19);
      post(lds, myland, LB0 + 20);
      TS();
      wait1(lds, C_MP + lw * 4, 40);
      f32x4 pv[6];
#pragma unroll
      for (int t = 0; t < 6; ++t) pv[t] = xch[(lw * 6 + t) * 64 + lane];
      post(lds, myhp, 4);
      SB();
#pragma unroll
      for (int t = 0; t < 6; ++t)
#pragma unroll
        for (int i = 0; i < 4; ++i) o[4 * t + i] += pv[t][i] * rstd;
#pragma unroll
      for (int t = 0; t < 3; ++t) {
        mishN<8>(o + 8 * t);
        const v4u af = {pk2(o[8 * t], o[8 * t + 1]), pk2(o[8 * t + 2], o[8 * t + 3]), pk2(o[8 * t + 4], o[8 * t + 5]), pk2(o[8 * t + 6], o[8 * t + 7])};
        ST128(clean4(af), rw, (t < ntile) ? lane * 16 : OOB, WS_ACT + (c32 * 86 + 2 * (tile0 + t) + 1) * 1024);
        SB();
      }
    }
    TS();
    wait4(lds, C_MP, 40);
    putW(20); putW(21); putW(22); putW(23); post(lds, myland, LB0 + 24);
    TS();
    wait4(lds, C_MP, 54);
    putW(24); putW(25); putW(26); putW(27); putW(28); post(lds, myland, LB0 + 29);
    TS();
    TSDUMP();
    return;
  }

  const int mymp = C_MP + w * 4;
  v2u xres[4];
  unsigned xs[4];
#pragma unroll
  for (int s = 0; s < 4; ++s) {
    xs[s] = ldsb + xrow * 128 + (((2 * s + h2) ^ xsw) * 16);
    asm volatile("" : "+v"(xs[s]));
  }
  unsigned w1r[8];
  const LB tn = mkbase(ldsb + (8 * h2 + q4) * 128 + ((kh ^ (q4 >> 1)) * 64) + trcol);
  const unsigned tlo = __builtin_amdgcn_readfirstlane((unsigned)e), thi = __builtin_amdgcn_readfirstlane((unsigned)(e >> 32));
  const rsrc_t rw = mkrw(tlo);
  {
    if (tid == 0) *(LAS v2u*)(lds + C_TAG) = v2u{tlo, thi};
    TS();
    float mean[2], var[2];
    float ee[2][8];
#pragma unroll
    for (int i = 0; i < 2; ++i) {
#pragma unroll
      for (int j = 0; j < 4; ++j) { ee[i][2 * j] = bflo(xr[i][j]); ee[i][2 * j + 1] = bfhi(xr[i][j]); }
      mean[i] = ((ee[i][0] + ee[i][1]) + (ee[i][2] + ee[i][3])) + ((ee[i][4] + ee[i][5]) + (ee[i][6] + ee[i][7]));
    }
#pragma unroll
    for (int i = 0; i < 2; ++i) mean[i] = wave_sum(mean[i]) * (1.f / 512.f);
#pragma unroll
    for (int i = 0; i < 2; ++i) {
      float v = 0.f;
#pragma unroll
      for (int j = 0; j < 8; ++j) { const float d = ee[i][j] - mean[i]; v += d * d; }
      var[i] = v;
    }
#pragma unroll
    for (int i = 0; i < 2; ++i) var[i] = wave_sum(var[i]) * (1.f / 512.f);
    {
      const bool odd = lane & 1;
      const v4u st = {asu(odd ? mean[1] : mean[0]), tlo, asu(__builtin_amdgcn_rsqf((odd ? var[1] : var[0]) + 1e-5f)), thi};
      ST128(st, rw, lane < 2 ? lane * 16 : OOB, WS_XST + (panel * 64 + 8 * mem + 2 * w) * 16);
    }
    TS();
    {
      float a[8];
#pragma unroll
      for (int j = 0; j < 4; ++j) { a[2 * j] = bflo(wr[0][j]) + bflo(wr[1][j]); a[2 * j + 1] = bfhi(wr[0][j]) + bfhi(wr[1][j]); }
#pragma unroll
      for (int j = 0; j < 8; ++j) a[j] = half_sum(row_pair_sum(dpp_add<0x128>(a[j])));
      if (lane < 8) {
        LAS f32x4* t = (LAS f32x4*)(lds + L_RED + 4096 + w * 256 + lane * 32);
        t[0] = f32x4{a[0], a[1], a[2], a[3]}; t[1] = f32x4{a[4], a[5], a[6], a[7]};
      }
    }
    MEMB();
    if (arrive_last(lds, C_CNT + 4, 4, lane)) {
      MEMB();
      const LAS f32x2* t2 = (const LAS f32x2*)(lds + L_RED + 4096) + r32;
      const f32x2 a = (t2[0] + t2[32]) + (t2[64] + t2[96]);
      const v4u o = {asu(a[0]), tlo, asu(a[1]), thi};
      ST128(o, rw, (lane < 32 && cb < 3) ? lane * 16 : OOB, WS_CSQ + ((mem * 3 + cb) * 8 + rb) * 512);
    }
    MEMB();
    TS();
    {
      const v4u pz = {POISON, POISON, POISON, POISON};
      const int so = (m32 * 32 + 4 * mem + 2 * kh) * 1024;
      ST128(pz, rw, lane * 16, WS_PQ + so);
      ST128(pz, rw, lane * 16, WS_PQ + so + 1024);
      ST128(pz, rw, lane * 16, WS_H + so);
      ST128(pz, rw, lane * 16, WS_H + so + 1024);
      ST128(pz, rw, lane < 16 ? lane * 16 : OOB, WS_HS + m32 * 4096 + (mem * 2 + kh) * 256);
      MEMB();
    }
    {
      const rsrc_t rout = __builtin_amdgcn_make_buffer_rsrc((void*)p.out, 0, S_ * D_ * 2, 0x00020000);
      const int one = lane == 0 ? 0 : OOB;
      if (w == 0) glds(rw, L_WARM, one, WS_S1 + (mem * 32 + panel) * 4096);
      if (w == 2) glds(rout, L_WARM, one, panel * 65536);
    }
  }
  TS();

  {
    f32x16 ak = {}, av = {};
    float rr[16], nn[16], csk = 0.f, csv = 0.f;
    v4u gcs[4];
    const int gco = w < 3 ? WS_CSQ + (mem * 3 + w) * 4096 : WS_XST + panel * 1024;
    const int gcv = w < 3 ? h2 * 2048 + r32 * 16 : lane * 16, gstr = w < 3 ? 512 : 0;
    auto ldG = [&]() LAM {
#pragma unroll
      for (int i = 0; i < 4; ++i) gcs[i] = LD128(rw, gcv, gco + i * gstr);
    };
    auto okG = [&]() LAM {
      bool ok = true;
#pragma unroll
      for (int i = 0; i < 4; ++i) ok = ok && gcs[i][1] == tlo && gcs[i][3] == thi;
      return all_lanes(ok);
    };
    v4u xf[5], wk[5], wv[5];
    auto rdx = [&](int s) LAM { return *(const LAS v4u*)((const LAS char*)(size_t)xs[s & 3] + (s >> 2) * 8192); };
    unsigned landed = wait4(lds, C_LAND, 1);
    TS();
#pragma unroll
    for (int s = 0; s < 4; ++s) {
      xf[s] = rdx(s);
      wk[s] = tr_frag(tn, wa_of(0) + s * 2048, 512);
      wv[s] = tr_frag(tn, wa_of(0) + 8192 + s * 2048, 512);
    }
    post(lds, mymp, 1);
    v4u lnext;
#pragma unroll
    for (int s = 0; s < 32; ++s) {
      const int c = s >> 2, n = s + 4;
      if ((s & 3) == 0 && n < 32) {
        if (s > 0) landed = min4(lnext);
        if (landed < (unsigned)(c + 2)) landed = wait4(lds, C_LAND, c + 2);
      }
      ak = MFMA32(xf[s % 5], wk[s % 5], ak);
      if (n < 32) { xf[n % 5] = rdx(n); wk[n % 5] = tr_frag(tn, wa_of(n >> 2) + (n & 3) * 2048, 512); }
      SB();
      av = MFMA32(xf[s % 5], wv[s % 5], av);
      if (n < 32) wv[n % 5] = tr_frag(tn, wa_of(n >> 2) + 8192 + (n & 3) * 2048, 512);
      if ((s & 3) == 1 && n < 28) lnext = *(volatile LAS v4u*)(lds + C_LAND);
      SB();
      if (s == GQ0) ldG();
      if (s == GQ1 && !okG()) { MEMB(); ldG(); }
      if (s == 23) {
#pragma nounroll
        for (unsigned sp = 0; sp < SPIN_CAP; ++sp) {
          if (okG()) break;
          MEMB();
          ldG();
        }
        float a0 = (asf(gcs[0][0]) + asf(gcs[1][0])) + (asf(gcs[2][0]) + asf(gcs[3][0]));
        float a1 = (asf(gcs[0][2]) + asf(gcs[1][2])) + (asf(gcs[2][2]) + asf(gcs[3][2]));
        a0 = half_sum(a0); a1 = half_sum(a1);
        if (w == 3) *(LAS v2u*)(lds + L_ST + lane * 8) = v2u{gcs[0][0], gcs[0][2]};
        else if (lane < 32) *(LAS f32x2*)(lds + L_CS + w * 256 + lane * 8) = f32x2{a0, a1};
      }
      if ((s & 3) == 3 && n < 32) post(lds, mymp, c + 2);
      if (s == 26) {
        wait4(lds, C_MP, 7);
        if (w == 0) ST64((v2u{tlo, thi}), rw, lane == 0 ? 0 : OOB, WS_FLAGP + pp * 128 + cm * 8);
        const LAS float* cp = (const LAS float*)(lds + L_CS);
        csk = cp[64 + kh * 32 + r32]; csv = cp[128 + kh * 32 + r32];
#pragma unroll
        for (int q = 0; q < 4; ++q) {
          const LAS f32x4* sp = (const LAS f32x4*)(lds + L_ST + (mg * 32 + 8 * q + 4 * h2) * 8);
          const f32x4 s01 = sp[0], s23 = sp[1];
          rr[4 * q] = s01[1]; rr[4 * q + 1] = s01[3]; rr[4 * q + 2] = s23[1]; rr[4 * q + 3] = s23[3];
          nn[4 * q] = -s01[0]; nn[4 * q + 1] = -s01[2]; nn[4 * q + 2] = -s23[0]; nn[4 * q + 3] = -s23[2];
        }
      }
    }
    TS();
    float kk[16];
#pragma unroll
    for (int i = 0; i < 16; ++i) kk[i] = (ak[i] + nn[i] * csk) * rr[i];
    phi16(kk);
    v4u pkf[2], vf[2];
#pragma unroll
    for (int i = 0; i < 8; ++i) {
      pkf[i >> 2][i & 3] = pk2(kk[2 * i], kk[2 * i + 1]);
      vf[i >> 2][i & 3] = pk2((av[2 * i] + nn[2 * i] * csv) * rr[2 * i], (av[2 * i + 1] + nn[2 * i + 1] * csv) * rr[2 * i + 1]);
    }
    f32x16 kv = {};
    kv = MFMA32(pkf[0], vf[0], kv);
    kv = MFMA32(pkf[1], vf[1], kv);
    const float zs = (((kk[0] + kk[1]) + (kk[2] + kk[3])) + ((kk[4] + kk[5]) + (kk[6] + kk[7]))) +
                     (((kk[8] + kk[9]) + (kk[10] + kk[11])) + ((kk[12] + kk[13]) + (kk[14] + kk[15])));
    LAS float* red = (LAS float*)(lds + L_RED);
    {
      const bool hi = (lane & 16) != 0;
      float lo8[8], hi8[8];
#pragma unroll
      for (int i = 0; i < 8; ++i) { lo8[i] = kv[i]; hi8[i] = kv[8 + i]; asm volatile("" : "+v"(lo8[i]), "+v"(hi8[i])); }
      LAS float* dst = red + mg * 1024 + kh * 512 + (hi ? 256 : 0) + h2 * 64 + (lane & 15) * 4;
#pragma unroll
      for (int pp = 0; pp < 2; ++pp) {
        f32x4 v;
#pragma unroll
        for (int i = 0; i < 4; ++i) v[i] = hi ? hi8[4 * pp + i] : lo8[4 * pp + i];
        *(LAS f32x4*)(dst + pp * 128) = v;
      }
      red[2048 + (mg * 2 + h2) * 64 + kh * 32 + r32] = zs;
    }
    post(lds, mymp, 9);
    wait1(lds, C_MP + (w ^ 1) * 4, 9);
    {
      const unsigned ta = tagA(tlo);
      const int hl = 2 * kh + mg;
      const f32x4 v = *(const LAS f32x4*)(red + hl * 256 + lane * 4) + *(const LAS f32x4*)(red + 1024 + hl * 256 + lane * 4);
      const LAS f32x4* zp = (const LAS f32x4*)(red + 2048 + hl * 16 + (lane & 3) * 4);
      const f32x4 zv = (zp[0] + zp[16]) + (zp[32] + zp[48]);
      ST128(tag24(v, ta), rw, (lane >> 3) * 4096 + (lane & 7) * 16, WS_S1 + (mem * 32 + hl * 8) * 4096 + panel * 128);
      ST128(tag24(zv, ta), rw, lane < 4 ? lane * 512 : OOB, WS_S1Z + (mem * 16 + hl * 4) * 512 + panel * 16);
      MEMB();
    }
    v2u fp = LD64(rw, (lane & 15) * 8, WS_FLAGP + pp * 128);
#pragma unroll
    for (int i = 0; i < 8; ++i) w1r[i] = __builtin_amdgcn_raw_buffer_load_b32(r1, lane < ntile * 16 ? lane * 4 : OOB, (32 * pp + 8 * w + i) * 2752 + tile0 * 64, 0);
    TS();
    f32x16 aq = {}, aq2 = {};
    {
      wait4(lds, C_LAND, 12);
      constexpr int QD = 6;
      v4u xq[QD + 1], wq[QD + 1];
      auto rdq = [&](int s) LAM { xq[s % (QD + 1)] = rdx(s); wq[s % (QD + 1)] = tr_frag(tn, wa_of(8 + (s >> 3)) + (s & 7) * 2048, 512); };
#pragma unroll
      for (int s = 0; s < QD; ++s) rdq(s);
      SB();
#pragma unroll
      for (int s = 0; s < 32; ++s) {
        if (s & 1) aq2 = MFMA32(wq[s % (QD + 1)], xq[s % (QD + 1)], aq2);
        else aq = MFMA32(wq[s % (QD + 1)], xq[s % (QD + 1)], aq);
        if (s + QD < 32) rdq(s + QD);
        if (s + QD == 31) post(lds, mymp, 10);
        SB();
      }
    }
    const f32x2 st = *(const LAS f32x2*)(lds + L_ST + xrow * 8);
    const float csq = *(const LAS float*)(lds + L_CS + (kh * 32 + r32) * 4);
    aq = rank1(h2 ? 0.f : csq, -st[0], aq);
    float t[16];
#pragma unroll
    for (int i = 0; i < 16; ++i) t[i] = (aq[i] + aq2[i]) * st[1];
    phi16(t);
    const v4u f0 = {pk2(t[0], t[1]), pk2(t[2], t[3]), pk2(t[4], t[5]), pk2(t[6], t[7])};
    const v4u f1 = {pk2(t[8], t[9]), pk2(t[10], t[11]), pk2(t[12], t[13]), pk2(t[14], t[15])};
    SB();
    TS();
    const int so = WS_PQ + (m32 * 32 + 4 * mem + 2 * kh) * 1024;
    ST128(clean4(f0), rw, lane * 16, so);
    ST128(clean4(f1), rw, lane * 16, so + 1024);
    MEMB();
#pragma nounroll
    for (unsigned sp = 0; sp < SPIN_CAP; ++sp) {
      if (all_lanes(fp.x == tlo && fp.y == thi)) break;
      MEMB();
      __builtin_amdgcn_s_sleep(PSLEEP);
      fp = LD64(rw, (lane & 15) * 8, WS_FLAGP + pp * 128);
    }
    MEMB();
#pragma unroll
    for (int q = 0; q < 4; ++q)
      xres[q] = __builtin_amdgcn_raw_buffer_load_b64(rx, r32 * 1024 + h2 * 8 + q * 16, (panel * 64 + mg * 32) * 1024 + (mem * 64 + kh * 32) * 2, 0);
    TS();
  }

  const LB ta0 = mkbase(ldsb + (4 * h2 + q4) * 128 + ((q4 >> 1) ^ kh) * 64 + trcol + kh * 6144);
  const LB ta1 = mkbase(ldsb + (4 * h2 + q4) * 128 + ((q4 >> 1) ^ kh ^ 1) * 64 + trcol + kh * 6144);
  v4u hres[2];
  {
    v4u kl[4][2], zl[2];
    const int kvv = h2 * 1024 + r32 * 16, kvs = WS_KV + xcd * KVREP;
    const int zv = lane < 16 ? (lane >> 1) * 64 + (lane & 1) * 16 : OOB;
    auto ldKV = [&]() LAM {
#pragma unroll
      for (int i = 0; i < 4; ++i) {
        kl[i][0] = LD128(rw, kvv, kvs + (8 * w + 2 * i) * 1024);
        kl[i][1] = LD128(rw, kvv, kvs + (8 * w + 2 * i) * 1024 + 512);
      }
      zl[0] = LD128(rw, zv, kvs + KVZ + w * 512);
      zl[1] = LD128(rw, zv, kvs + KVZ + w * 512 + 32);
    };
    const unsigned ta = tagA(tlo), tb = tagB(tlo);
    __builtin_amdgcn_s_sleep(QSLEEP);
    TS();
    v4u pq[16];
#pragma unroll
    for (int i = 0; i < 16; ++i) pq[i] = LD128(rw, lane * 16, WS_PQ + (m32 * 32 + 16 * kh + i) * 1024);
    SB();
    TS();
    {
      float a0 = 0.f, a1 = 0.f;
#pragma unroll
      for (int i = 0; i < 8; ++i) { a0 += bflo(w1r[i]); a1 += bfhi(w1r[i]); }
      wait4(lds, C_MP, 10);
      ((LAS f32x2*)(lds + L_C1))[w * 64 + lane] = f32x2{a0, a1};
      MEMB();
      if (arrive_last(lds, C_CNT, 4, lane)) {
        MEMB();
        const LAS f32x4* t = (const LAS f32x4*)(lds + L_C1) + (lane & 31);
        ST128(tag24((t[0] + t[32]) + (t[64] + t[96]), ta), rw, (lane < ntile * 8) ? lane * 16 : OOB, WS_CS1 + pp * 5632 + tile0 * 128);
      }
      MEMB();
    }
#pragma nounroll
    for (unsigned sp = 0; sp < SPIN_CAP; ++sp) {
      bool bad[8], any = false;
#pragma unroll
      for (int i = 0; i < 8; ++i) { bad[i] = umax(max4(pq[2 * i]), max4(pq[2 * i + 1])) == POISON; any |= bad[i]; }
      if (all_lanes(!any)) break;
      MEMB();
      __builtin_amdgcn_s_sleep(PSLEEP);
#pragma unroll
      for (int i = 0; i < 8; ++i)
        if (bad[i]) {
          pq[2 * i] = LD128(rw, lane * 16, WS_PQ + (m32 * 32 + 16 * kh + 2 * i) * 1024);
          pq[2 * i + 1] = LD128(rw, lane * 16, WS_PQ + (m32 * 32 + 16 * kh + 2 * i + 1) * 1024);
        }
    }
    TS();
    ldKV();
#pragma nounroll
    for (unsigned sp = 0; sp < SPIN_CAP; ++sp) {
      unsigned bad[5];
#pragma unroll
      for (int i = 0; i < 4; ++i) bad[i] = bad16(kl[i][0], ta, tb) | bad16(kl[i][1], ta, tb);
      bad[4] = lane < 16 ? (bad16(zl[0], ta, tb) | bad16(zl[1], ta, tb)) : 0u;
      if (all_lanes((((bad[0] | bad[1]) | (bad[2] | bad[3])) | bad[4]) == 0u)) break;
      MEMB();
#pragma unroll
      for (int i = 0; i < 4; ++i)
        if (bad[i]) {
          kl[i][0] = LD128(rw, kvv, kvs + (8 * w + 2 * i) * 1024);
          kl[i][1] = LD128(rw, kvv, kvs + (8 * w + 2 * i) * 1024 + 512);
        }
      if (bad[4]) {
        zl[0] = LD128(rw, zv, kvs + KVZ + w * 512);
        zl[1] = LD128(rw, zv, kvs + KVZ + w * 512 + 32);
      }
    }
    TS();
    {
      const v4u pz = {POISON, POISON, POISON, POISON};
#pragma unroll
      for (int t = 0; t < 6; ++t) ST128(pz, rw, (t < 2 * ntile) ? lane * 16 : OOB, WS_ACT + ((pp * 4 + w) * 86 + 2 * tile0 + t) * 1024);
      MEMB();
    }
    {
      auto cv = [&](v4u x, v4u y) LAM { return v4u{hi16s(x[0], x[1]), hi16s(x[2], x[3]), hi16s(y[0], y[1]), hi16s(y[2], y[3])}; };
#pragma unroll
      for (int i = 0; i < 4; ++i) *(LAS v4u*)(lds + L_X + (8 * w + 2 * i) * 512 + lane * 16) = cv(kl[i][0], kl[i][1]);
      if (lane < 16) *(LAS v4u*)(lds + L_KZ + w * 256 + lane * 16) = cv(zl[0], zl[1]);
    }
    post(lds, mymp, 12);
    wait4(lds, C_LAND, LB0 + 6);
    v4u wf[3][2];
#pragma unroll
    for (int i = 0; i < 2; ++i) { wf[i][0] = tr_frag(ta0, slot_of(0) + i * 2048, 1024); wf[i][1] = tr_frag(ta1, slot_of(0) + i * 2048, 1024); }
    wait1(lds, C_MP + (w ^ 1) * 4, 12);
    v4u kf[16];
    {
      const int kb = (r32 < 16) ? L_X + kh * 8192 + (h2 * 16 + r32) * 16 : L_KZ + kh * 512 + h2 * 16;
      const int ks = (r32 < 16) ? 512 : 32;
#pragma unroll
      for (int i = 0; i < 16; ++i) kf[i] = *(const LAS v4u*)(lds + kb + i * ks);
    }
    post(lds, mymp, 13);
    TS();
    f32x16 acc[2] = {};
    f32x16 o[2];
    v4u of[2];
    const f32x16 zero = {};
    auto scale_lo = [&](const f32x16& oc, float& inv, v4u& dst) LAM {
      inv = __builtin_amdgcn_rcpf(oc[8] + 1e-6f);
      dst[0] = pk2(oc[0] * inv, oc[1] * inv); dst[1] = pk2(oc[2] * inv, oc[3] * inv);
    };
    auto scale_hi = [&](const f32x16& oc, float inv, v4u& dst) LAM {
      dst[2] = pk2(oc[4] * inv, oc[5] * inv); dst[3] = pk2(oc[6] * inv, oc[7] * inv);
    };
    float inv;
    o[0] = MFMA32(kf[0], pq[0], zero);
    o[1] = MFMA32(kf[1], pq[1], zero);
    SB();
    scale_lo(o[0], inv, of[0]); scale_hi(o[0], inv, of[0]);
    SB();
#pragma unroll
    for (int hd = 0; hd < 16; ++hd) {
      acc[0] = MFMA32(wf[hd % 3][0], of[hd & 1], acc[0]);
      if (hd + 1 < 16) scale_lo(o[(hd + 1) & 1], inv, of[(hd + 1) & 1]);
      SB();
      acc[1] = MFMA32(wf[hd % 3][1], of[hd & 1], acc[1]);
      if (hd + 1 < 16) scale_hi(o[(hd + 1) & 1], inv, of[(hd + 1) & 1]);
      SB();
      if (hd + 2 < 16) {
        o[hd & 1] = MFMA32(kf[hd + 2], pq[hd + 2], zero);
        const int sl = slot_of((hd + 2) / 3) + ((hd + 2) % 3) * 2048;
        wf[(hd + 2) % 3][0] = tr_frag(ta0, sl, 1024);
        wf[(hd + 2) % 3][1] = tr_frag(ta1, sl, 1024);
      }
      SB();
    }
    TS();
    wait1(lds, C_MP + (w ^ 1) * 4, 13);
#pragma unroll
    for (int q = 0; q < 4; ++q) xch[(w * 4 + q) * 64 + lane] = f32x4{acc[1][4 * q], acc[1][4 * q + 1], acc[1][4 * q + 2], acc[1][4 * q + 3]};
    post(lds, mymp, 14);
    wait1(lds, C_MP + (w ^ 2) * 4, 14);
    float s1 = 0.f, s2 = 0.f;
#pragma unroll
    for (int q = 0; q < 4; ++q) {
      const f32x4 t = xch[((w ^ 2) * 4 + q) * 64 + lane];
      const unsigned ua = pk2(acc[0][4 * q] + t[0] + bflo(xres[q].x), acc[0][4 * q + 1] + t[1] + bfhi(xres[q].x));
      const unsigned ub = pk2(acc[0][4 * q + 2] + t[2] + bflo(xres[q].y), acc[0][4 * q + 3] + t[3] + bfhi(xres[q].y));
      hres[q >> 1][(q & 1) * 2] = ua;
      hres[q >> 1][(q & 1) * 2 + 1] = ub;
    }
    const int so = WS_H + (m32 * 32 + 4 * mem + 2 * kh) * 1024;
    post(lds, mymp, 15);
    hres[0] = clean4(hres[0]); hres[1] = clean4(hres[1]);
    SB(); WAITV(0);
    ST128(hres[0], rw, lane * 16, so);
    ST128(hres[1], rw, lane * 16, so + 1024);
    SB();
#pragma unroll
    for (int q = 0; q < 8; ++q) {
      const unsigned u = hres[q >> 2][q & 3];
      const float e0 = bflo(u), e1 = bfhi(u);
      s1 += e0 + e1;
      s2 += e0 * e0 + e1 * e1;
    }
    s1 = half_sum(s1); s2 = half_sum(s2);
    const v2u st = {clean(asu(s1)), clean(asu(s2))};
    ST64(st, rw, lane < 32 ? lane * 8 : OOB, WS_HS + m32 * 4096 + (mem * 2 + kh) * 256);
    TS();
  }

  {
    const LB tc = mkbase(ldsb + (4 * h2 + q4) * 64 + trcol);
    const int c32 = pp * 4 + w;
    v4u R[2][6];
    wait4(lds, C_LAND, LB0 + 10);
#pragma unroll
    for (int s = 0; s < 6; ++s) R[0][s] = tr_frag(tc, slot_of(6) + (s % 3) * 4096 + (s / 3) * 1024, 512);
    v4u hf[3][4];
    const int hso = WS_H + c32 * 32 * 1024;
    auto ldH = [&](int g) LAM {
#pragma unroll
      for (int i = 0; i < 4; ++i) hf[g % 3][i] = LD128(rw, lane * 16 + i * 1024, hso + kgC(g) * 4096);
    };
    auto okH = [&](int g) LAM {
      return all_lanes(umax(umax(max4(hf[g % 3][0]), max4(hf[g % 3][1])), umax(max4(hf[g % 3][2]), max4(hf[g % 3][3]))) != POISON);
    };
    __builtin_amdgcn_s_sleep(FIRSTSLEEP);
    ldH(0); ldH(1); ldH(2);
    SB();
    f32x16 acc[3] = {};
#pragma unroll
    for (int j = 0; j < 16; ++j) {
      if ((j & 1) == 0) {
        const int g = j >> 1;
        if (!okH(g)) {
#pragma nounroll
          for (unsigned sp = 0; sp < SPIN_CAP; ++sp) {
            MEMB();
            __builtin_amdgcn_s_sleep(PSLEEP);
            ldH(g);
            if (okH(g)) break;
          }
          if (g + 1 < 8) ldH(g + 1);
          if (g + 2 < 8) ldH(g + 2);
        }
        if (j == 6) wait4(lds, C_LAND, LB0 + 14);
        SB();
        if (j == 0) { post(lds, mymp, 20); TS(); }
      }
#pragma unroll
      for (int s = 0; s < 6; ++s) {
        const int ks = 2 * j + s / 3, tt = s % 3;
        if (j + 1 < 16) R[(j + 1) & 1][s] = tr_frag(tc, slot_of(6 + ((j + 1) >> 1)) + tt * 4096 + (((j + 1) & 1) * 2 + s / 3) * 1024, 512);
        acc[tt] = MFMA32(R[j & 1][s], hf[(ks >> 2) % 3][ks & 3], acc[tt]);
        if (tt == 2 && ks + 12 < 32) hf[(ks >> 2) % 3][ks & 3] = LD128(rw, lane * 16 + (ks & 3) * 1024, hso + kgC((ks >> 2) + 3) * 4096);
        SB();
      }
      if ((j & 3) == 2) post(lds, mymp, 20 + (j >> 1));
    }
    TS();
    wait4(lds, C_MP, 15);
#pragma unroll
    for (int t = 0; t < 3; ++t) {
      xch[(w * 6 + 2 * t) * 64 + lane] = f32x4{acc[t][8], acc[t][9], acc[t][10], acc[t][11]};
      xch[(w * 6 + 2 * t + 1) * 64 + lane] = f32x4{acc[t][12], acc[t][13], acc[t][14], acc[t][15]};
    }
    post(lds, mymp, 40);
    wait4(lds, C_HP, 3);
    {
      const f32x2 st = *(const LAS f32x2*)(lds + L_ST2 + (w * 32 + r32) * 8);
      float o[24];
#pragma unroll
      for (int t = 0; t < 3; ++t)
#pragma unroll
        for (int qq = 0; qq < 2; ++qq) {
          const f32x4 cs = *(const LAS f32x4*)(lds + L_CS1 + (t * 32 + 8 * qq + 4 * h2) * 4);
          const f32x4 bb = *(const LAS f32x4*)(lds + L_B1 + (t * 32 + 8 * qq + 4 * h2) * 4);
#pragma unroll
          for (int i = 0; i < 4; ++i) o[8 * t + 4 * qq + i] = acc[t][4 * qq + i] * st[1] + (st[0] * cs[i] + bb[i]);
        }
#pragma unroll
      for (int t = 0; t < 3; ++t) {
        mishN<8>(o + 8 * t);
        const v4u af = {pk2(o[8 * t], o[8 * t + 1]), pk2(o[8 * t + 2], o[8 * t + 3]), pk2(o[8 * t + 4], o[8 * t + 5]), pk2(o[8 * t + 6], o[8 * t + 7])};
        ST128(clean4(af), rw, (t < ntile) ? lane * 16 : OOB, WS_ACT + (c32 * 86 + 2 * (tile0 + t)) * 1024);
        SB();
      }
    }
    TS();
  }

  {
    v4u R[2][6];
    unsigned landed = wait4(lds, C_LAND, LB0 + 16);
    wait4(lds, C_MP, 40);
#pragma unroll
    for (int s = 0; s < 3; ++s) { R[0][s] = tr_frag(ta0, slot_of(14) + s * 2048, 1024); R[0][3 + s] = tr_frag(ta1, slot_of(14) + s * 2048, 1024); }
    constexpr int NG = 5;
    v4u af[NG][3];
    const int aso = WS_ACT + (m32 * 86 + 43 * kh) * 1024;
    auto ldA = [&](int g) LAM {
#pragma unroll
      for (int i = 0; i < 3; ++i) if (3 * g + i < 43) af[g % NG][i] = LD128(rw, lane * 16, aso + ksD(3 * g + i) * 1024);
    };
    auto okA = [&](int g) LAM {
      unsigned m = max4(af[g % NG][0]);
      if (g < 14) m = umax(m, umax(max4(af[g % NG][1]), max4(af[g % NG][2])));
      return all_lanes(m != POISON);
    };
    __builtin_amdgcn_s_sleep(FIRSTSLEEP);
#pragma unroll
    for (int g = 0; g < NG; ++g) ldA(g);
    SB();
    f32x16 acc[2] = {};
#pragma unroll
    for (int c = 0; c < 15; ++c) {
      if (c + 1 < 15 && landed < (unsigned)(LB0 + 14 + c + 2)) landed = wait4(lds, C_LAND, LB0 + 14 + c + 2);
      if (!okA(c)) {
#pragma nounroll
        for (unsigned sp = 0; sp < SPIN_CAP; ++sp) {
          MEMB();
          __builtin_amdgcn_s_sleep(PSLEEP);
          ldA(c);
          if (okA(c)) break;
        }
#pragma unroll
        for (int g = c + 1; g < c + NG; ++g) if (g < 15) ldA(g);
      }
      SB();
      if (c == 0) TS();
#pragma unroll
      for (int s = 0; s < 3; ++s) {
        const int ks = c * 3 + s;
        if (ks + 3 < 43) {
          R[(c + 1) & 1][s] = tr_frag(ta0, slot_of(14 + c + 1) + s * 2048, 1024);
          R[(c + 1) & 1][3 + s] = tr_frag(ta1, slot_of(14 + c + 1) + s * 2048, 1024);
        }
        if (ks < 43) {
          const v4u a = af[c % NG][s];
          acc[0] = MFMA32(R[c & 1][s], a, acc[0]);
          acc[1] = MFMA32(R[c & 1][3 + s], a, acc[1]);
          if (ks + 3 * NG < 43) af[c % NG][s] = LD128(rw, lane * 16, aso + ksD(ks + 3 * NG) * 1024);
        }
        SB();
      }
      if (c == 3) post(lds, mymp, 54);
    }
    TS();
    wait4(lds, C_HP, 4);
#pragma unroll
    for (int q = 0; q < 4; ++q) xch[(w * 4 + q) * 64 + lane] = f32x4{acc[1][4 * q], acc[1][4 * q + 1], acc[1][4 * q + 2], acc[1][4 * q + 3]};
    post(lds, mymp, 70);
    wait1(lds, C_MP + (w ^ 2) * 4, 70);
    int lz = tid; asm volatile("" : "+v"(lz));
    const rsrc_t rout = __builtin_amdgcn_make_buffer_rsrc((void*)p.out, 0, S_ * D_ * 2, 0x00020000);
    const int oo = (lz & 31) * 1024 + ((lz >> 5) & 1) * 8;
    const int so = (panel * 64 + mg * 32) * 1024 + (mem * 64 + kh * 32) * 2;
#pragma unroll
    for (int q = 0; q < 4; ++q) {
      const f32x4 t = xch[((w ^ 2) * 4 + q) * 64 + lane];
      const unsigned ha = hres[q >> 1][(q & 1) * 2], hb = hres[q >> 1][(q & 1) * 2 + 1];
      v2u o;
      o.x = pk2(acc[0][4 * q] + t[0] + bflo(ha), acc[0][4 * q + 1] + t[1] + bfhi(ha));
      o.y = pk2(acc[0][4 * q + 2] + t[2] + bflo(hb), acc[0][4 * q + 3] + t[3] + bfhi(hb));
      __builtin_amdgcn_raw_buffer_store_b64(o, rout, oo + q * 16, so, OUTAUX);
    }
    TS();
    TSDUMP();
  }
}

extern "C" void kernel_launch(void* const* d_in, const int* in_sizes, int n_in, void* d_out, int out_size, void* d_ws, size_t ws_size,
                              hipStream_t stream) {
  Params p;
  p.x = (const char*)d_in[0]; p.wqkv = (const char*)d_in[1]; p.wo = (const char*)d_in[2];
  p.w1 = (const char*)d_in[4]; p.b1 = (const char*)d_in[5]; p.w2 = (const char*)d_in[6];
  p.out = (char*)d_out;
  unsigned long long nreg = NREG;
  while (nreg > 1 && nreg * (unsigned long long)RSTRIDE > ws_size) nreg >>= 1;
  p.wsr = (unsigned long long)(size_t)d_ws | (nreg - 1);
  (void)hipFuncSetAttribute((const void*)fused_block, hipFuncAttributeMaxDynamicSharedMemorySize, LDS_BYTES);
  fused_block<<<256, 512, LDS_BYTES, stream>>>(p);
}
```

```cpp
#define KSTAMP 99
#define PW 3
#include <hip/hip_runtime.h>
#include <hip/hip_bf16.h>
#include <stdint.h>

#if defined(__HIP_DEVICE_COMPILE__)
#define NOPK __attribute__((target("no-packed-fp32-ops")))
#else
#define NOPK
#endif
#define DI __device__ __forceinline__ NOPK
#define LAM NOPK __attribute__((always_inline))
#define SB() __builtin_amdgcn_sched_barrier(0)
#define MEMB() asm volatile("" ::: "memory")
#define LAS __attribute__((address_space(3)))
#define GAS __attribute__((address_space(1)))
typedef unsigned v4u __attribute__((ext_vector_type(4)));
typedef unsigned v2u __attribute__((ext_vector_type(2)));
typedef short s16x4 __attribute__((ext_vector_type(4)));
typedef __bf16 bf16x8 __attribute__((ext_vector_type(8)));
typedef __bf16 bf16x2 __attribute__((ext_vector_type(2)));
typedef float f32x16 __attribute__((ext_vector_type(16)));
typedef float f32x4 __attribute__((ext_vector_type(4)));
typedef float f32x2 __attribute__((ext_vector_type(2)));
typedef unsigned long long u64;
typedef __amdgpu_buffer_rsrc_t rsrc_t;

constexpr int S_ = 2048, D_ = 512, F_ = 1376;
constexpr int WS_EPOCH = 0;
constexpr int WS_FLAGP = 4096;
constexpr int WS_XST = 32768;
constexpr int WS_CSQ = 65536;
constexpr int WS_CS1 = 163840;
constexpr int WS_S1Z = 262144;
constexpr int WS_KV = 327680;
constexpr int KVREP = 36864, KVZ = 32768;
constexpr int WS_HS = 655360;
constexpr int WS_PQ = 1 << 20;
constexpr int WS_H = 3 << 20;
constexpr int WS_ACT = 5 << 20;
constexpr int WS_S1 = WS_ACT + 64 * 86 * 1024;
constexpr int WS_BYTES = WS_S1 + (1 << 20);
constexpr int CODE_KB = 45;
constexpr int WS_STAMP = 240 << 20;
constexpr int RSTRIDE = 12 << 20;
constexpr int OOB = 0x40000000;

constexpr int L_ST = 147456;
constexpr int L_CS = 147968;
constexpr int L_RED = 148992;
constexpr int SLOT = 12288, NSLOT = 10;
constexpr int L_X = 122880;
constexpr int L_KZ = L_X + 16384;
constexpr int L_C1 = L_X + 20480;
constexpr int L_ST2 = 159232;
constexpr int L_CS1 = 160256;
constexpr int L_B1 = 161024;
constexpr int L_WARM = 161792;
constexpr int L_CTL = 163584;
constexpr int C_LAND = L_CTL, C_MP = L_CTL + 16, C_HP = L_CTL + 32, C_GO = L_CTL + 48, C_CNT = L_CTL + 64, C_LAND2 = L_CTL + 96, C_TAG = L_CTL + 128;
constexpr int LDS_BYTES = 163840;
#define slot_of(c) (((c) % NSLOT) * SLOT)
#define wa_of(c) (65536 + ((c) % 5) * 16384)
constexpr int LB0 = 12;

constexpr int NSTG = 14;

#define ROT_A 2
#define CODEPF 1
#define ROT_C 0
#define ROT_D 0
#define W2S 0
#define NEARLY 4
#define KVORD 2
#define POIS 2
#define QSLEEP 8
#define NREG 16
#define GQ0 8
#define GQ1 15
#define WO3 1
#define RDLY 10
#define STG1 0
#define W69 0
#define RSLEEP 1
#define TOUCH 1
#define OUTAUX 0
#define STG 1
#define FIRSTSLEEP 8
#define MAP42 1
#define PSLEEP 2
#define LSLEEP 1
constexpr int AUX_SC1 = 16;
constexpr unsigned SPIN_CAP = 1u << 16;
constexpr unsigned LSPIN_CAP = 1u << 22;

#define MFMA32(a, b, c) __builtin_amdgcn_mfma_f32_32x32x16_bf16(__builtin_bit_cast(bf16x8, a), __builtin_bit_cast(bf16x8, b), c, 0, 0, 0)
#define WAITV_(n) asm volatile("s_waitcnt vmcnt(" #n ")" ::: "memory")
#define WAITV(n) WAITV_(n)
#define RLX_AGENT __ATOMIC_RELAXED, __HIP_MEMORY_SCOPE_AGENT
#define LD128(rs, vo, so) __builtin_amdgcn_raw_buffer_load_b128(rs, vo, so, AUX_SC1)
#define LD64(rs, vo, so) __builtin_amdgcn_raw_buffer_load_b64(rs, vo, so, AUX_SC1)
#define ST128(v, rs, vo, so) __builtin_amdgcn_raw_buffer_store_b128(v, rs, (vo) + (so), 0, AUX_SC1)
#define ST64(v, rs, vo, so) __builtin_amdgcn_raw_buffer_store_b64(v, rs, vo, so, AUX_SC1)

DI float asf(unsigned u) { return __builtin_bit_cast(float, u); }
DI unsigned asu(float f) { return __builtin_bit_cast(unsigned, f); }
DI unsigned pk2(float a, float b) {
  f32x2 f = {a, b};
  return __builtin_bit_cast(unsigned, __builtin_convertvector(f, bf16x2));
}
DI float bflo(unsigned u) { return asf(u << 16); }
DI float bfhi(unsigned u) { return asf(u & 0xffff0000u); }
struct LB { unsigned a[3]; };
DI LB mkbase(unsigned a) {
  LB r; r.a[0] = a; r.a[1] = a + 65536u; r.a[2] = a + 131072u;
  asm volatile("" : "+v"(r.a[0]), "+v"(r.a[1]), "+v"(r.a[2]));
  return r;
}
DI const LAS char* lp(const LB& b, int off) { return (const LAS char*)(size_t)b.a[off >> 16] + (off & 65535); }
DI v4u tr_frag(const LB& b, int off, int dt) {
  s16x4 x = __builtin_amdgcn_ds_read_tr16_b64_v4i16((LAS s16x4*)lp(b, off));
  s16x4 y = __builtin_amdgcn_ds_read_tr16_b64_v4i16((LAS s16x4*)lp(b, off + dt));
  v2u ux = __builtin_bit_cast(v2u, x), uy = __builtin_bit_cast(v2u, y);
  return v4u{ux.x, ux.y, uy.x, uy.y};
}
DI float half_sum(float v) {
  auto r = __builtin_amdgcn_permlane32_swap(asu(v), asu(v), false, false);
  const unsigned r0 = r[0], r1 = r[1];
  return asf(r0) + asf(r1);
}
DI float row_pair_sum(float v) {
  auto r = __builtin_amdgcn_permlane16_swap(asu(v), asu(v), false, false);
  const unsigned r0 = r[0], r1 = r[1];
  return asf(r0) + asf(r1);
}
template <int CTRL> DI float dpp_add(float v) { return v + asf(__builtin_amdgcn_update_dpp(0, asu(v), CTRL, 0xf, 0xf, true)); }
DI float wave_sum(float v) {
  v = dpp_add<0xB1>(v); v = dpp_add<0x4E>(v); v = dpp_add<0x141>(v); v = dpp_add<0x140>(v);
  return half_sum(row_pair_sum(v));
}
constexpr unsigned POISON = 0xffffffffu;
DI unsigned umax(unsigned a, unsigned b) { return a > b ? a : b; }
DI unsigned clean(unsigned d) { return d < 0xfffffffeu ? d : 0xfffffffeu; }
DI v4u clean4(v4u v) { return v4u{clean(v.x), clean(v.y), clean(v.z), clean(v.w)}; }
DI unsigned max4(v4u v) { return umax(umax(v.x, v.y), umax(v.z, v.w)); }
DI bool all_lanes(bool ok) { return __builtin_amdgcn_ballot_w64(!ok) == 0ull; }
DI unsigned tagA(unsigned tlo) { return tlo * 0x9E3779B1u; }
DI unsigned tagB(unsigned tlo) { return tlo * 0x85EBCA77u; }
DI v4u tag24(f32x4 v, unsigned t) {
  v4u r;
#pragma unroll
  for (int i = 0; i < 4; ++i) r[i] = ((asu(v[i]) + 0x80u) & 0xffffff00u) | ((t >> (8 * i)) & 0xffu);
  return r;
}
DI unsigned bad24(v4u g, unsigned t) { return ((g.x ^ t) | (g.y ^ (t >> 8)) | (g.z ^ (t >> 16)) | (g.w ^ (t >> 24))) & 0xffu; }
DI f32x4 val24(v4u g) { return f32x4{asf(g.x & 0xffffff00u), asf(g.y & 0xffffff00u), asf(g.z & 0xffffff00u), asf(g.w & 0xffffff00u)}; }
DI v4u tag16(f32x4 v, unsigned ta, unsigned tb) {
  const unsigned p0 = pk2(v[0], v[1]), p1 = pk2(v[2], v[3]);
  return v4u{(p0 << 16) | (ta & 0xffffu), (p0 & 0xffff0000u) | (ta >> 16), (p1 << 16) | (tb & 0xffffu), (p1 & 0xffff0000u) | (tb >> 16)};
}
DI unsigned lo16s(unsigned a, unsigned b) { return __builtin_amdgcn_perm(b, a, 0x05040100u); }
DI unsigned hi16s(unsigned a, unsigned b) { return __builtin_amdgcn_perm(b, a, 0x07060302u); }
DI unsigned bad16(v4u g, unsigned ta, unsigned tb) { return (lo16s(g.x, g.y) ^ ta) | (lo16s(g.z, g.w) ^ tb); }
DI f32x16 rank1(float a, float b, f32x16 c) {
  asm volatile("s_nop 7\n\ts_nop 7" : "+v"(c));
  c = __builtin_amdgcn_mfma_f32_32x32x2f32(a, b, c, 0, 0, 0);
  return c;
}
constexpr float LOG2E = 1.44269504088896341f;
DI void phi16(float (&t)[16]) {
  float e[16];
#pragma unroll
  for (int i = 0; i < 16; ++i) e[i] = __builtin_fminf(t[i], 0.f) * LOG2E;
  SB();
#pragma unroll
  for (int i = 0; i < 16; ++i) { e[i] = __builtin_amdgcn_exp2f(e[i]); t[i] = __builtin_fmaxf(t[i], 0.f) + 1e-6f; }
  SB();
#pragma unroll
  for (int i = 0; i < 16; ++i) t[i] += e[i];
}
constexpr float LN2 = 0.693147180559945309f;
template <int N> DI void mishN(float* a) {
  float u[N];
#pragma unroll
  for (int i = 0; i < N; ++i) u[i] = __builtin_amdgcn_exp2f(a[i]) + 1.f;
#pragma unroll
  for (int i = 0; i < N; ++i) u[i] = __builtin_amdgcn_rcpf(u[i] * u[i] + 1.f);
#pragma unroll
  for (int i = 0; i < N; ++i) a[i] = a[i] * (LN2 - (2.f * LN2) * u[i]);
}
DI void glds(rsrc_t rs, int ldsoff, int voff, int soff) {
  asm volatile("s_mov_b32 m0, %0\n\ts_nop 3\n\tbuffer_load_dwordx4 %1, %2, %3 offen lds" ::"s"(ldsoff), "v"(voff), "s"(rs), "s"(soff) : "memory");
}
DI u64 ld_flag(const char* ws, int off) { return __hip_atomic_load((const GAS u64*)(ws + off), RLX_AGENT); }
DI void st_flag(char* ws, int off, u64 v) { __hip_atomic_store((GAS u64*)(ws + off), v, RLX_AGENT); }
DI void wait_flags(const char* ws, int off, u64 tag) {
#pragma nounroll
  for (unsigned sp = 0; sp < SPIN_CAP; ++sp) {
    const bool ok = ld_flag(ws, off) == tag;
    if (all_lanes(ok)) break;
    __builtin_amdgcn_s_sleep(PSLEEP);
  }
  __builtin_amdgcn_fence(__ATOMIC_ACQUIRE, "wavefront");
}
DI unsigned min4(v4u v) {
  const unsigned a = v.x < v.y ? v.x : v.y, b = v.z < v.w ? v.z : v.w;
  return __builtin_amdgcn_readfirstlane(a < b ? a : b);
}
DI unsigned wait4(LAS char* lds, int off, unsigned e) {
  unsigned m = 0;
  SB();
#pragma nounroll
  for (unsigned sp = 0; sp < LSPIN_CAP; ++sp) {
    m = min4(*(volatile LAS v4u*)(lds + off));
    if (m >= e) break;
    __builtin_amdgcn_s_sleep(LSLEEP);
  }
  MEMB(); SB();
  return m;
}
DI void wait1(LAS char* lds, int off, unsigned e) {
  SB();
#pragma nounroll
  for (unsigned sp = 0; sp < LSPIN_CAP; ++sp) {
    if (__builtin_amdgcn_readfirstlane(*(volatile LAS unsigned*)(lds + off)) >= e) break;
    __builtin_amdgcn_s_sleep(LSLEEP);
  }
  MEMB(); SB();
}
DI void post(LAS char* lds, int off, unsigned v) {
  SB(); MEMB();
  *(volatile LAS unsigned*)(lds + off) = v;
  MEMB(); SB();
}
DI bool arrive_last(LAS char* lds, int off, unsigned n, int lane) {
  unsigned old = 0;
  if (lane == 0) old = __hip_atomic_fetch_add((LAS unsigned*)(lds + off), 1u, __ATOMIC_RELAXED, __HIP_MEMORY_SCOPE_WORKGROUP);
  return __builtin_amdgcn_readfirstlane(old) == n - 1;
}

#define TS() do { if (tsn == KSTAMP) { SB(); tk = __builtin_amdgcn_s_memrealtime(); SB(); } tsn++; } while (0)
#define TSDUMP() do { if (b == 100 && w == PW) { const u64 dl = 3000ull + (tk - tstart); if (dl < 8000ull) while (__builtin_amdgcn_s_memrealtime() - tstart < dl) __builtin_amdgcn_s_sleep(1); } } while (0)

struct Params {
  const char* x; const char* wqkv; const char* wo; const char* w1; const char* b1; const char* w2;
  char* out; u64 wsr;
};

__global__ __launch_bounds__(512, 2) NOPK void fused_block(Params p) {
  extern __shared__ __attribute__((aligned(16))) char smem[];
  LAS char* lds = (LAS char*)smem;
  const u64 tstart = __builtin_amdgcn_s_memrealtime();
  const int tid = threadIdx.x, lane = tid & 63;
  const int w = __builtin_amdgcn_readfirstlane(tid >> 6);
  const int b = blockIdx.x, xcd = b & 7, loc = b >> 3;
  if (tid < 32) *(LAS unsigned*)(lds + L_CTL + tid * 4) = 0u;
  const u64 pc0 = __builtin_amdgcn_s_getpc() & ~255ull;
  auto codepf = [&](int piece) LAM {
    const rsrc_t rc = __builtin_amdgcn_make_buffer_rsrc((void*)pc0, 0, CODE_KB * 1024, 0x00020000);
    glds(rc, L_WARM, lane * 16, piece * 1024);
  };
  codepf((loc & 7) * 8 + w);
  const int panel = (xcd >> 1) * 8 + (loc >> 2), mem = (xcd & 1) * 4 + (loc & 3);
  const int pp = panel >> 1, cm = (panel & 1) * 8 + mem;
  const int ntile = cm < 11 ? 3 : 2, tile0 = min(3 * cm, 2 * cm + 11);
  char* ws = (char*)(p.wsr & ~255ull);
  int tsn = 0; u64 tk = tstart;
  const int h2 = lane >> 5, r32 = lane & 31;
  const int w4 = w & 3;
  TS();
  const int mg = w & 1, kh = w4 >> 1;
  const int m32 = panel * 2 + mg;
  auto mkrw = [&](unsigned tl) LAM { return __builtin_amdgcn_make_buffer_rsrc((void*)(ws + (size_t)(tl & (unsigned)p.wsr & 255u) * RSTRIDE), 0, WS_BYTES, 0x00020000); };
  const rsrc_t rx = __builtin_amdgcn_make_buffer_rsrc((void*)p.x, 0, S_ * D_ * 2, 0x00020000);
  const rsrc_t rq = __builtin_amdgcn_make_buffer_rsrc((void*)p.wqkv, 0, D_ * 3 * D_ * 2, 0x00020000);
  const rsrc_t r1 = __builtin_amdgcn_make_buffer_rsrc((void*)p.w1, 0, D_ * F_ * 2, 0x00020000);
  const unsigned ldsb = (unsigned)(size_t)lds;
  const int q4 = (lane & 15) >> 2;
  const int trcol = ((lane >> 4) & 1) * 32 + (lane & 3) * 8;
  const int xrow = mg * 32 + r32;
  const int xsw = (xrow >> 1) & 7;
  LAS f32x4* xch = (LAS f32x4*)(lds + L_X);
  const int rot = ROT_A == 1 ? (mem + panel) & 7 : ROT_A == 2 ? (2 * mem + panel) & 7 : 0;
  const int rotc = ROT_C ? 2 * (mem & 3) + (panel & 1) : 0, rotd = ROT_D ? 11 * (mem & 3) : 0;
  auto kgC = [&](int g) LAM { return (g + rotc) & 7; };
  auto ksD = [&](int k) LAM { const int t = k + rotd; return t >= 43 ? t - 43 : t; };

  const int lw = w4;
    const rsrc_t ro = __builtin_amdgcn_make_buffer_rsrc((void*)p.wo, 0, D_ * D_ * 2, 0x00020000);
    const rsrc_t r2 = __builtin_amdgcn_make_buffer_rsrc((void*)p.w2, 0, F_ * D_ * 2, 0x00020000);
    const int wsw = ((lane & 7) ^ (((lane >> 4) & 1) << 2)) * 16;
    const int vo = (lane >> 3) * 1024 + wsw;
    const int v1 = (lane >> 2) * 2752 + (lane & 3) * 16;
    const int v1x = ntile == 3 ? v1 : OOB;
    auto pieceW = [&](int c, int u) LAM {
      const int sl = slot_of(c);
      if (c < 6) {
        const int q = lw + 4 * u, k2 = q >= 6 ? 1 : 0, i8 = q - 6 * k2;
        glds(ro, sl + q * 1024, (c == 5 && i8 >= 2) ? OOB : vo, (k2 * 256 + 48 * c + 8 * i8) * 1024 + mem * 128);
      } else if (c < 14) {
        glds(r1, sl + u * 4096 + lw * 1024, u < 2 ? v1 : v1x, (64 * kgC(c - 6) + 16 * lw) * 2752 + (tile0 + u) * 64);
      }
    };
    auto issueW = [&](int c) LAM {
#pragma unroll
      for (int u = 0; u < 3; ++u) pieceW(c, u);
    };
  const int myland = C_LAND + lw * 4;

    auto mkvx = [&](int l) LAM { return (l >> 3) * 1024 + (((l & 7) ^ (((lw & 1) * 4 + (l >> 4)) & 7)) * 16); };
    auto mkvq = [&](int l) LAM { return (l >> 3) * 3072 + ((l & 7) ^ (((l >> 4) & 1) << 2)) * 16; };
    auto issueA = [&](int c, int vx, int vq) LAM {
      if (c < 8) {
        const int kc = (c + rot) & 7;
#pragma unroll
        for (int j = 0; j < 2; ++j) { const int i = lw + 4 * j; glds(rx, c * 8192 + i * 1024, vx, (panel * 64 + 8 * i) * 1024 + kc * 128); }
#pragma unroll
        for (int j = 0; j < 2; ++j)
#pragma unroll
          for (int t = 0; t < 2; ++t) {
            const int i = lw + 4 * j;
            glds(rq, wa_of(c) + t * 8192 + i * 1024, vq, (kc * 64 + 8 * i) * 3072 + (t + 1) * 1024 + mem * 128);
          }
      } else {
#pragma unroll
        for (int t = 0; t < 2; ++t) {
          const int kc = (2 * (c - 8) + t + rot) & 7;
#pragma unroll
          for (int j = 0; j < 2; ++j) { const int i = lw + 4 * j; glds(rq, wa_of(c) + t * 8192 + i * 1024, vq, (kc * 64 + 8 * i) * 3072 + mem * 128); }
        }
      }
    };
  const int cb = panel >> 3, rb = panel & 7;
  u64 e;
  v4u xr[2], wr[2];
  const int vx0 = mkvx(lane), vq0 = mkvq(lane);
    unsigned mp = 0;
    v4u mpn;
    auto peek = [&]() LAM { mpn = *(volatile LAS v4u*)(lds + C_MP); };
    auto need = [&](unsigned e) LAM { mp = min4(mpn); if (mp < e) mp = wait4(lds, C_MP, e); };
  TS();
  { const char* px = p.x; asm volatile("" :: "s"(px)); }
  asm volatile("s_waitcnt lgkmcnt(0)\n\ts_barrier" ::: "memory");
  TS();
  if (w >= 4) {
    issueA(0, vx0, vq0);
  } else {
    e = ld_flag(ws, WS_EPOCH) + 1ull;
#pragma unroll
    for (int i = 0; i < 2; ++i) xr[i] = __builtin_amdgcn_raw_buffer_load_b128(rx, lane * 16, (panel * 64 + 8 * mem + 2 * w + i) * 1024, 0);
    const int vq2 = (cb < 3) ? (lane >> 3) * 3072 + (lane & 7) * 16 : OOB;
#pragma unroll
    for (int i = 0; i < 2; ++i) wr[i] = __builtin_amdgcn_raw_buffer_load_b128(rq, vq2, (64 * rb + 16 * w + 8 * i) * 3072 + cb * 1024 + mem * 128, 0);
  }
  SB(); MEMB();
  TS();
  if (__builtin_expect(w >= 4, 1)) {
    issueA(1, vx0, vq0); issueA(2, vx0, vq0);
    TS();
    WAITV(12); post(lds, myland, 1); issueA(3, vx0, vq0);
    TS();
    WAITV(12); post(lds, myland, 2); issueA(4, vx0, vq0);
  }
  int w2 = w;
  asm volatile("" : "+s"(w2));
  if (w2 >= 4) {
    const int myhp = C_HP + lw * 4;
    int l2 = tid; asm volatile("" : "+v"(l2));
    const int vx = mkvx(l2 & 63), vq = mkvq(l2 & 63);
    v4u stg[NSTG][3];
    auto ldW = [&](int c) LAM {
#pragma unroll
      for (int u = 0; u < 3; ++u) {
        if (c < 6) {
          const int q = lw + 4 * u, k2 = q >= 6 ? 1 : 0, i8 = q - 6 * k2;
          stg[4 + c][u] = __builtin_amdgcn_raw_buffer_load_b128(ro, (c == 5 && i8 >= 2) ? OOB : vo, (k2 * 256 + 48 * c + 8 * i8) * 1024 + mem * 128, 0);
        } else if (c < 14) {
          stg[(c - 10) % NSTG][u] = __builtin_amdgcn_raw_buffer_load_b128(r1, u < 2 ? v1 : v1x, (64 * kgC(c - 6) + 16 * lw) * 2752 + (tile0 + u) * 64, 0);
        } else if (3 * (c - 14) + u < 43) {
          stg[(c - 10) % NSTG][u] = __builtin_amdgcn_raw_buffer_load_b128(r2, vo, (kh * 688 + 16 * ksD(3 * (c - 14) + u) + 8 * mg) * 1024 + mem * 128, 0);
        }
      }
      MEMB();
    };
    const LB pw1 = mkbase(ldsb + lw * 1024 + lane * 16);
    const LB pw2 = mkbase(ldsb + kh * 6144 + mg * 1024 + lane * 16);
    auto putW = [&](int c) LAM {
#pragma unroll
      for (int u = 0; u < 3; ++u) {
        if (c < 6) *(LAS v4u*)lp(pw1, slot_of(c) + u * 4096) = stg[4 + c][u];
        else if (c < 14) *(LAS v4u*)lp(pw1, slot_of(c) + u * 4096) = stg[(c - 10) % NSTG][u];
        else if (3 * (c - 14) + u < 43) *(LAS v4u*)lp(pw2, slot_of(c) + u * 2048) = stg[(c - 10) % NSTG][u];
      }
    };
    peek();
    WAITV(12); post(lds, myland, 3); need(1); issueA(5, vx, vq); peek();
    WAITV(12); post(lds, myland, 4); need(2); issueA(6, vx, vq); peek();
    WAITV(12); post(lds, myland, 5); need(3); issueA(7, vx, vq); peek();
    WAITV(12); post(lds, myland, 6); need(4); issueA(8, vx, vq); peek();
    WAITV(10); post(lds, myland, 7); need(5); issueA(9, vx, vq); peek();
    WAITV(8); post(lds, myland, 8);
    TS();
    if (WO3 || lw != 3) { ldW(0); ldW(1); ldW(2); ldW(3); ldW(4); ldW(5); }
    need(7); issueA(10, vx, vq); issueA(11, vx, vq);
    SB(); WAITV(0); post(lds, myland, 12);
    TS();
    u64 tag;
    unsigned tl;
    {
      const v2u tg = *(const LAS v2u*)(lds + C_TAG);
      tl = __builtin_amdgcn_readfirstlane(tg.x);
      const unsigned th = __builtin_amdgcn_readfirstlane(tg.y);
      tag = (u64)tl | ((u64)th << 32);
    }
    const rsrc_t rw = mkrw(tl);
    if (lw == 3) {
      const unsigned ta = tagA(tl), tb = tagB(tl);
      const int so = WS_S1 + (mem * 32 + panel) * 4096, soz = WS_S1Z + (mem * 16 + (panel >> 1)) * 512;
      const bool zr = (panel & 1) == 0;
      v4u g[5];
      wait4(lds, C_MP, RDLY);
#pragma unroll
      for (int i = 0; i < 4; ++i) g[i] = LD128(rw, lane * 16, so + i * 1024);
      g[4] = LD128(rw, zr ? r32 * 16 : OOB, soz);
#pragma nounroll
      for (unsigned sp = 0; sp < SPIN_CAP; ++sp) {
        unsigned bad[5];
#pragma unroll
        for (int i = 0; i < 4; ++i) bad[i] = bad24(g[i], ta);
        bad[4] = zr ? bad24(g[4], ta) : 0u;
        if (all_lanes((((bad[0] | bad[1]) | (bad[2] | bad[3])) | bad[4]) == 0u)) break;
        MEMB();
        __builtin_amdgcn_s_sleep(RSLEEP);
#pragma unroll
        for (int i = 0; i < 4; ++i) if (bad[i]) g[i] = LD128(rw, lane * 16, so + i * 1024);
        if (bad[4]) g[4] = LD128(rw, r32 * 16, soz);
      }
      f32x4 a = (val24(g[0]) + val24(g[1])) + (val24(g[2]) + val24(g[3]));
#pragma unroll
      for (int i = 0; i < 4; ++i) a[i] = half_sum(row_pair_sum(dpp_add<0x128>(a[i])));
      ST128(tag16(a, ta, tb), rw, (lane >> 3) * KVREP + (lane & 7) * 16, WS_KV + mem * 4096 + panel * 128);
      MEMB();
      a = val24(g[4]);
#pragma unroll
      for (int i = 0; i < 4; ++i) a[i] = row_pair_sum(dpp_add<0x140>(dpp_add<0x141>(dpp_add<0x4E>(dpp_add<0xB1>(a[i])))));
      ST128(tag16(a, ta, tb), rw, (zr && lane < 8) ? lane * KVREP : OOB, WS_KV + KVZ + (mem * 16 + (panel >> 1)) * 16);
      MEMB();
      if (!WO3) { ldW(0); ldW(1); ldW(2); ldW(3); ldW(4); ldW(5); }
    } else {
      ldW(10); ldW(11); ldW(12); ldW(13);
    }
    TS();
    wait4(lds, C_MP, 10);
    putW(0); putW(1); putW(2); putW(3); putW(4); putW(5);
    post(lds, myland, LB0 + 6);
    issueW(6); issueW(7); issueW(8); issueW(9);
    TS();
    wait4(lds, C_MP, 12);
    if (b == 0 && lw == 0 && lane == 0) st_flag(ws, WS_EPOCH, tag);
    TS();
    {
      v4u cl[4];
      const int vo1 = ((lane & 15) < 6 && lw * 6 + (lane & 15) < ntile * 8) ? (lane >> 4) * 4 * 5632 + (lane & 15) * 16 : OOB;
#pragma unroll
      for (int i = 0; i < 4; ++i) cl[i] = LD128(rw, vo1, WS_CS1 + i * 5632 + tile0 * 128 + lw * 96);
      const rsrc_t rb = __builtin_amdgcn_make_buffer_rsrc((void*)p.b1, 0, F_ * 2, 0x00020000);
      const v2u bb = __builtin_amdgcn_raw_buffer_load_b64(rb, tile0 * 64 + lw * 48 + lane * 8, 0, 0);
      SB(); WAITV(5);
      post(lds, myland, LB0 + 10);
      const unsigned ta = tagA(tl);
#pragma nounroll
      for (unsigned sp = 0; sp < SPIN_CAP; ++sp) {
        if (all_lanes(vo1 == OOB || ((bad24(cl[0], ta) | bad24(cl[1], ta)) | (bad24(cl[2], ta) | bad24(cl[3], ta))) == 0u)) break;
        MEMB();
        __builtin_amdgcn_s_sleep(PSLEEP);
#pragma unroll
        for (int i = 0; i < 4; ++i) cl[i] = LD128(rw, vo1, WS_CS1 + i * 5632 + tile0 * 128 + lw * 96);
      }
      f32x4 a = (val24(cl[0]) + val24(cl[1])) + (val24(cl[2]) + val24(cl[3]));
#pragma unroll
      for (int i = 0; i < 4; ++i) a[i] = half_sum(row_pair_sum(a[i]));
      if (lane < 6) {
        *(LAS f32x4*)(lds + L_CS1 + lw * 96 + lane * 16) = a;
        *(LAS f32x4*)(lds + L_B1 + lw * 96 + lane * 16) = f32x4{bflo(bb.x) * LOG2E, bfhi(bb.x) * LOG2E, bflo(bb.y) * LOG2E, bfhi(bb.y) * LOG2E};
      }
      post(lds, myhp, 2);
    }
    if (lw == 3 || NEARLY < 4) { ldW(10); ldW(11); ldW(12); ldW(13); }
    ldW(14); ldW(15); ldW(16); ldW(17); ldW(18); ldW(19);
    ldW(20); ldW(21); ldW(22); ldW(23);
    wait4(lds, C_MP, 14);
    putW(10); putW(11); putW(12); putW(13);
    post(lds, myland, LB0 + 14);
    TS();
    ldW(24); ldW(25); ldW(26); ldW(27);
    TS();
    {
      const int c32 = pp * 4 + lw;
      v2u hs[8];
      wait1(lds, C_MP + lw * 4, 20);
#pragma nounroll
      for (unsigned sp = 0; sp < SPIN_CAP; ++sp) {
        unsigned m = 0;
#pragma unroll
        for (int i = 0; i < 8; ++i) hs[i] = LD64(rw, (8 * h2 + i) * 256 + r32 * 8, WS_HS + c32 * 4096);
#pragma unroll
        for (int i = 0; i < 8; ++i) m = umax(m, umax(hs[i].x, hs[i].y));
        if (all_lanes(m != POISON)) break;
        MEMB();
        __builtin_amdgcn_s_sleep(PSLEEP);
      }
      float s1 = 0.f, s2 = 0.f;
#pragma unroll
      for (int i = 0; i < 8; ++i) { s1 += asf(hs[i].x); s2 += asf(hs[i].y); }
      s1 = half_sum(s1); s2 = half_sum(s2);
      const float mu = s1 * (1.f / 512.f);
      const float rstd = __builtin_amdgcn_rsqf(__builtin_fmaxf(s2 * (1.f / 512.f) - mu * mu, 0.f) + 1e-5f) * LOG2E;
      const float nm = -mu * rstd;
      if (lane < 32) *(LAS f32x2*)(lds + L_ST2 + (lw * 32 + r32) * 8) = f32x2{nm, rstd};
      post(lds, myhp, 3);
      wait4(lds, C_HP, 2);
      float o[24];
#pragma unroll
      for (int t = 0; t < 3; ++t)
#pragma unroll
        for (int qq = 0; qq < 2; ++qq) {
          const f32x4 cs = *(const LAS f32x4*)(lds + L_CS1 + (t * 32 + 16 + 8 * qq + 4 * h2) * 4);
          const f32x4 bb = *(const LAS f32x4*)(lds + L_B1 + (t * 32 + 16 + 8 * qq + 4 * h2) * 4);
#pragma unroll
          for (int i = 0; i < 4; ++i) o[8 * t + 4 * qq + i] = nm * cs[i] + bb[i];
        }
#pragma unroll
      for (int i = 0; i < 24; ++i) asm volatile("" : "+v"(o[i]));
      putW(14); putW(15);
      ldW(28);
      wait4(lds, C_MP, 21); putW(16); putW(17);
      wait4(lds, C_MP, 23); putW(18); putW(19);
      post(lds, myland, LB0 + 20);
      TS();
      wait1(lds, C_MP + lw * 4, 40);
      f32x4 pv[6];
#pragma unroll
      for (int t = 0; t < 6; ++t) pv[t] = xch[(lw * 6 + t) * 64 + lane];
      post(lds, myhp, 4);
      SB();
#pragma unroll
      for (int t = 0; t < 6; ++t)
#pragma unroll
        for (int i = 0; i < 4; ++i) o[4 * t + i] += pv[t][i] * rstd;
#pragma unroll
      for (int t = 0; t < 3; ++t) {
        mishN<8>(o + 8 * t);
        const v4u af = {pk2(o[8 * t], o[8 * t + 1]), pk2(o[8 * t + 2], o[8 * t + 3]), pk2(o[8 * t + 4], o[8 * t + 5]), pk2(o[8 * t + 6], o[8 * t + 7])};
        ST128(clean4(af), rw, (t < ntile) ? lane * 16 : OOB, WS_ACT + (c32 * 86 + 2 * (tile0 + t) + 1) * 1024);
        SB();
      }
    }
    TS();
    wait4(lds, C_MP, 40);
    putW(20); putW(21); putW(22); putW(23); post(lds, myland, LB0 + 24);
    TS();
    wait4(lds, C_MP, 54);
    putW(24); putW(25); putW(26); putW(27); putW(28); post(lds, myland, LB0 + 29);
    TS();
    TSDUMP();
    return;
  }

  const int mymp = C_MP + w * 4;
  v2u xres[4];
  unsigned xs[4];
#pragma unroll
  for (int s = 0; s < 4; ++s) {
    xs[s] = ldsb + xrow * 128 + (((2 * s + h2) ^ xsw) * 16);
    asm volatile("" : "+v"(xs[s]));
  }
  unsigned w1r[8];
  const LB tn = mkbase(ldsb + (8 * h2 + q4) * 128 + ((kh ^ (q4 >> 1)) * 64) + trcol);
  const unsigned tlo = __builtin_amdgcn_readfirstlane((unsigned)e), thi = __builtin_amdgcn_readfirstlane((unsigned)(e >> 32));
  const rsrc_t rw = mkrw(tlo);
  {
    if (tid == 0) *(LAS v2u*)(lds + C_TAG) = v2u{tlo, thi};
    TS();
    float mean[2], var[2];
    float ee[2][8];
#pragma unroll
    for (int i = 0; i < 2; ++i) {
#pragma unroll
      for (int j = 0; j < 4; ++j) { ee[i][2 * j] = bflo(xr[i][j]); ee[i][2 * j + 1] = bfhi(xr[i][j]); }
      mean[i] = ((ee[i][0] + ee[i][1]) + (ee[i][2] + ee[i][3])) + ((ee[i][4] + ee[i][5]) + (ee[i][6] + ee[i][7]));
    }
#pragma unroll
    for (int i = 0; i < 2; ++i) mean[i] = wave_sum(mean[i]) * (1.f / 512.f);
#pragma unroll
    for (int i = 0; i < 2; ++i) {
      float v = 0.f;
#pragma unroll
      for (int j = 0; j < 8; ++j) { const float d = ee[i][j] - mean[i]; v += d * d; }
      var[i] = v;
    }
#pragma unroll
    for (int i = 0; i < 2; ++i) var[i] = wave_sum(var[i]) * (1.f / 512.f);
    {
      const bool odd = lane & 1;
      const v4u st = {asu(odd ? mean[1] : mean[0]), tlo, asu(__builtin_amdgcn_rsqf((odd ? var[1] : var[0]) + 1e-5f)), thi};
      ST128(st, rw, lane < 2 ? lane * 16 : OOB, WS_XST + (panel * 64 + 8 * mem + 2 * w) * 16);
    }
    TS();
    {
      float a[8];
#pragma unroll
      for (int j = 0; j < 4; ++j) { a[2 * j] = bflo(wr[0][j]) + bflo(wr[1][j]); a[2 * j + 1] = bfhi(wr[0][j]) + bfhi(wr[1][j]); }
#pragma unroll
      for (int j = 0; j < 8; ++j) a[j] = half_sum(row_pair_sum(dpp_add<0x128>(a[j])));
      if (lane < 8) {
        LAS f32x4* t = (LAS f32x4*)(lds + L_RED + 4096 + w * 256 + lane * 32);
        t[0] = f32x4{a[0], a[1], a[2], a[3]}; t[1] = f32x4{a[4], a[5], a[6], a[7]};
      }
    }
    MEMB();
    if (arrive_last(lds, C_CNT + 4, 4, lane)) {
      MEMB();
      const LAS f32x2* t2 = (const LAS f32x2*)(lds + L_RED + 4096) + r32;
      const f32x2 a = (t2[0] + t2[32]) + (t2[64] + t2[96]);
      const v4u o = {asu(a[0]), tlo, asu(a[1]), thi};
      ST128(o, rw, (lane < 32 && cb < 3) ? lane * 16 : OOB, WS_CSQ + ((mem * 3 + cb) * 8 + rb) * 512);
    }
    MEMB();
    TS();
    {
      const v4u pz = {POISON, POISON, POISON, POISON};
      const int so = (m32 * 32 + 4 * mem + 2 * kh) * 1024;
      ST128(pz, rw, lane * 16, WS_PQ + so);
      ST128(pz, rw, lane * 16, WS_PQ + so + 1024);
      ST128(pz, rw, lane * 16, WS_H + so);
      ST128(pz, rw, lane * 16, WS_H + so + 1024);
      ST128(pz, rw, lane < 16 ? lane * 16 : OOB, WS_HS + m32 * 4096 + (mem * 2 + kh) * 256);
      MEMB();
    }
    {
      const rsrc_t rout = __builtin_amdgcn_make_buffer_rsrc((void*)p.out, 0, S_ * D_ * 2, 0x00020000);
      const int one = lane == 0 ? 0 : OOB;
      if (w == 0) glds(rw, L_WARM, one, WS_S1 + (mem * 32 + panel) * 4096);
      if (w == 2) glds(rout, L_WARM, one, panel * 65536);
    }
  }
  TS();

  {
    f32x16 ak = {}, av = {};
    float rr[16], nn[16], csk = 0.f, csv = 0.f;
    v4u gcs[4];
    const int gco = w < 3 ? WS_CSQ + (mem * 3 + w) * 4096 : WS_XST + panel * 1024;
    const int gcv = w < 3 ? h2 * 2048 + r32 * 16 : lane * 16, gstr = w < 3 ? 512 : 0;
    auto ldG = [&]() LAM {
#pragma unroll
      for (int i = 0; i < 4; ++i) gcs[i] = LD128(rw, gcv, gco + i * gstr);
    };
    auto okG = [&]() LAM {
      bool ok = true;
#pragma unroll
      for (int i = 0; i < 4; ++i) ok = ok && gcs[i][1] == tlo && gcs[i][3] == thi;
      return all_lanes(ok);
    };
    v4u xf[5], wk[5], wv[5];
    auto rdx = [&](int s) LAM { return *(const LAS v4u*)((const LAS char*)(size_t)xs[s & 3] + (s >> 2) * 8192); };
    unsigned landed = wait4(lds, C_LAND, 1);
    TS();
#pragma unroll
    for (int s = 0; s < 4; ++s) {
      xf[s] = rdx(s);
      wk[s] = tr_frag(tn, wa_of(0) + s * 2048, 512);
      wv[s] = tr_frag(tn, wa_of(0) + 8192 + s * 2048, 512);
    }
    post(lds, mymp, 1);
    v4u lnext;
#pragma unroll
    for (int s = 0; s < 32; ++s) {
      const int c = s >> 2, n = s + 4;
      if ((s & 3) == 0 && n < 32) {
        if (s > 0) landed = min4(lnext);
        if (landed < (unsigned)(c + 2)) landed = wait4(lds, C_LAND, c + 2);
      }
      ak = MFMA32(xf[s % 5], wk[s % 5], ak);
      if (n < 32) { xf[n % 5] = rdx(n); wk[n % 5] = tr_frag(tn, wa_of(n >> 2) + (n & 3) * 2048, 512); }
      SB();
      av = MFMA32(xf[s % 5], wv[s % 5], av);
      if (n < 32) wv[n % 5] = tr_frag(tn, wa_of(n >> 2) + 8192 + (n & 3) * 2048, 512);
      if ((s & 3) == 1 && n < 28) lnext = *(volatile LAS v4u*)(lds + C_LAND);
      SB();
      if (s == GQ0) ldG();
      if (s == GQ1 && !okG()) { MEMB(); ldG(); }
      if (s == 23) {
#pragma nounroll
        for (unsigned sp = 0; sp < SPIN_CAP; ++sp) {
          if (okG()) break;
          MEMB();
          ldG();
        }
        float a0 = (asf(gcs[0][0]) + asf(gcs[1][0])) + (asf(gcs[2][0]) + asf(gcs[3][0]));
        float a1 = (asf(gcs[0][2]) + asf(gcs[1][2])) + (asf(gcs[2][2]) + asf(gcs[3][2]));
        a0 = half_sum(a0); a1 = half_sum(a1);
        if (w == 3) *(LAS v2u*)(lds + L_ST + lane * 8) = v2u{gcs[0][0], gcs[0][2]};
        else if (lane < 32) *(LAS f32x2*)(lds + L_CS + w * 256 + lane * 8) = f32x2{a0, a1};
      }
      if ((s & 3) == 3 && n < 32) post(lds, mymp, c + 2);
      if (s == 26) {
        wait4(lds, C_MP, 7);
        if (w == 0) ST64((v2u{tlo, thi}), rw, lane == 0 ? 0 : OOB, WS_FLAGP + pp * 128 + cm * 8);
        const LAS float* cp = (const LAS float*)(lds + L_CS);
        csk = cp[64 + kh * 32 + r32]; csv = cp[128 + kh * 32 + r32];
#pragma unroll
        for (int q = 0; q < 4; ++q) {
          const LAS f32x4* sp = (const LAS f32x4*)(lds + L_ST + (mg * 32 + 8 * q + 4 * h2) * 8);
          const f32x4 s01 = sp[0], s23 = sp[1];
          rr[4 * q] = s01[1]; rr[4 * q + 1] = s01[3]; rr[4 * q + 2] = s23[1]; rr[4 * q + 3] = s23[3];
          nn[4 * q] = -s01[0]; nn[4 * q + 1] = -s01[2]; nn[4 * q + 2] = -s23[0]; nn[4 * q + 3] = -s23[2];
        }
      }
    }
    TS();
    float kk[16];
#pragma unroll
    for (int i = 0; i < 16; ++i) kk[i] = (ak[i] + nn[i] * csk) * rr[i];
    phi16(kk);
    v4u pkf[2], vf[2];
#pragma unroll
    for (int i = 0; i < 8; ++i) {
      pkf[i >> 2][i & 3] = pk2(kk[2 * i], kk[2 * i + 1]);
      vf[i >> 2][i & 3] = pk2((av[2 * i] + nn[2 * i] * csv) * rr[2 * i], (av[2 * i + 1] + nn[2 * i + 1] * csv) * rr[2 * i + 1]);
    }
    f32x16 kv = {};
    kv = MFMA32(pkf[0], vf[0], kv);
    kv = MFMA32(pkf[1], vf[1], kv);
    const float zs = (((kk[0] + kk[1]) + (kk[2] + kk[3])) + ((kk[4] + kk[5]) + (kk[6] + kk[7]))) +
                     (((kk[8] + kk[9]) + (kk[10] + kk[11])) + ((kk[12] + kk[13]) + (kk[14] + kk[15])));
    LAS float* red = (LAS float*)(lds + L_RED);
    {
      const bool hi = (lane & 16) != 0;
      float lo8[8], hi8[8];
#pragma unroll
      for (int i = 0; i < 8; ++i) { lo8[i] = kv[i]; hi8[i] = kv[8 + i]; asm volatile("" : "+v"(lo8[i]), "+v"(hi8[i])); }
      LAS float* dst = red + mg * 1024 + kh * 512 + (hi ? 256 : 0) + h2 * 64 + (lane & 15) * 4;
#pragma unroll
      for (int pp = 0; pp < 2; ++pp) {
        f32x4 v;
#pragma unroll
        for (int i = 0; i < 4; ++i) v[i] = hi ? hi8[4 * pp + i] : lo8[4 * pp + i];
        *(LAS f32x4*)(dst + pp * 128) = v;
      }
      red[2048 + (mg * 2 + h2) * 64 + kh * 32 + r32] = zs;
    }
    post(lds, mymp, 9);
    wait1(lds, C_MP + (w ^ 1) * 4, 9);
    {
      const unsigned ta = tagA(tlo);
      const int hl = 2 * kh + mg;
      const f32x4 v = *(const LAS f32x4*)(red + hl * 256 + lane * 4) + *(const LAS f32x4*)(red + 1024 + hl * 256 + lane * 4);
      const LAS f32x4* zp = (const LAS f32x4*)(red + 2048 + hl * 16 + (lane & 3) * 4);
      const f32x4 zv = (zp[0] + zp[16]) + (zp[32] + zp[48]);
      ST128(tag24(v, ta), rw, (lane >> 3) * 4096 + (lane & 7) * 16, WS_S1 + (mem * 32 + hl * 8) * 4096 + panel * 128);
      ST128(tag24(zv, ta), rw, lane < 4 ? lane * 512 : OOB, WS_S1Z + (mem * 16 + hl * 4) * 512 + panel * 16);
      MEMB();
    }
    v2u fp = LD64(rw, (lane & 15) * 8, WS_FLAGP + pp * 128);
#pragma unroll
    for (int i = 0; i < 8; ++i) w1r[i] = __builtin_amdgcn_raw_buffer_load_b32(r1, lane < ntile * 16 ? lane * 4 : OOB, (32 * pp + 8 * w + i) * 2752 + tile0 * 64, 0);
    TS();
    f32x16 aq = {}, aq2 = {};
    {
      wait4(lds, C_LAND, 12);
      constexpr int QD = 6;
      v4u xq[QD + 1], wq[QD + 1];
      auto rdq = [&](int s) LAM { xq[s % (QD + 1)] = rdx(s); wq[s % (QD + 1)] = tr_frag(tn, wa_of(8 + (s >> 3)) + (s & 7) * 2048, 512); };
#pragma unroll
      for (int s = 0; s < QD; ++s) rdq(s);
      SB();
#pragma unroll
      for (int s = 0; s < 32; ++s) {
        if (s & 1) aq2 = MFMA32(wq[s % (QD + 1)], xq[s % (QD + 1)], aq2);
        else aq = MFMA32(wq[s % (QD + 1)], xq[s % (QD + 1)], aq);
        if (s + QD < 32) rdq(s + QD);
        if (s + QD == 31) post(lds, mymp, 10);
        SB();
      }
    }
    const f32x2 st = *(const LAS f32x2*)(lds + L_ST + xrow * 8);
    const float csq = *(const LAS float*)(lds + L_CS + (kh * 32 + r32) * 4);
    aq = rank1(h2 ? 0.f : csq, -st[0], aq);
    float t[16];
#pragma unroll
    for (int i = 0; i < 16; ++i) t[i] = (aq[i] + aq2[i]) * st[1];
    phi16(t);
    const v4u f0 = {pk2(t[0], t[1]), pk2(t[2], t[3]), pk2(t[4], t[5]), pk2(t[6], t[7])};
    const v4u f1 = {pk2(t[8], t[9]), pk2(t[10], t[11]), pk2(t[12], t[13]), pk2(t[14], t[15])};
    SB();
    TS();
    const int so = WS_PQ + (m32 * 32 + 4 * mem + 2 * kh) * 1024;
    ST128(clean4(f0), rw, lane * 16, so);
    ST128(clean4(f1), rw, lane * 16, so + 1024);
    MEMB();
#pragma nounroll
    for (unsigned sp = 0; sp < SPIN_CAP; ++sp) {
      if (all_lanes(fp.x == tlo && fp.y == thi)) break;
      MEMB();
      __builtin_amdgcn_s_sleep(PSLEEP);
      fp = LD64(rw, (lane & 15) * 8, WS_FLAGP + pp * 128);
    }
    MEMB();
#pragma unroll
    for (int q = 0; q < 4; ++q)
      xres[q] = __builtin_amdgcn_raw_buffer_load_b64(rx, r32 * 1024 + h2 * 8 + q * 16, (panel * 64 + mg * 32) * 1024 + (mem * 64 + kh * 32) * 2, 0);
    TS();
  }

  const LB ta0 = mkbase(ldsb + (4 * h2 + q4) * 128 + ((q4 >> 1) ^ kh) * 64 + trcol + kh * 6144);
  const LB ta1 = mkbase(ldsb + (4 * h2 + q4) * 128 + ((q4 >> 1) ^ kh ^ 1) * 64 + trcol + kh * 6144);
  v4u hres[2];
  {
    v4u kl[4][2], zl[2];
    const int kvv = h2 * 1024 + r32 * 16, kvs = WS_KV + xcd * KVREP;
    const int zv = lane < 16 ? (lane >> 1) * 64 + (lane & 1) * 16 : OOB;
    auto ldKV = [&]() LAM {
#pragma unroll
      for (int i = 0; i < 4; ++i) {
        kl[i][0] = LD128(rw, kvv, kvs + (8 * w + 2 * i) * 1024);
        kl[i][1] = LD128(rw, kvv, kvs + (8 * w + 2 * i) * 1024 + 512);
      }
      zl[0] = LD128(rw, zv, kvs + KVZ + w * 512);
      zl[1] = LD128(rw, zv, kvs + KVZ + w * 512 + 32);
    };
    const unsigned ta = tagA(tlo), tb = tagB(tlo);
    __builtin_amdgcn_s_sleep(QSLEEP);
    TS();
    v4u pq[16];
#pragma unroll
    for (int i = 0; i < 16; ++i) pq[i] = LD128(rw, lane * 16, WS_PQ + (m32 * 32 + 16 * kh + i) * 1024);
    SB();
    TS();
    {
      float a0 = 0.f, a1 = 0.f;
#pragma unroll
      for (int i = 0; i < 8; ++i) { a0 += bflo(w1r[i]); a1 += bfhi(w1r[i]); }
      wait4(lds, C_MP, 10);
      ((LAS f32x2*)(lds + L_C1))[w * 64 + lane] = f32x2{a0, a1};
      MEMB();
      if (arrive_last(lds, C_CNT, 4, lane)) {
        MEMB();
        const LAS f32x4* t = (const LAS f32x4*)(lds + L_C1) + (lane & 31);
        ST128(tag24((t[0] + t[32]) + (t[64] + t[96]), ta), rw, (lane < ntile * 8) ? lane * 16 : OOB, WS_CS1 + pp * 5632 + tile0 * 128);
      }
      MEMB();
    }
#pragma nounroll
    for (unsigned sp = 0; sp < SPIN_CAP; ++sp) {
      bool bad[8], any = false;
#pragma unroll
      for (int i = 0; i < 8; ++i) { bad[i] = umax(max4(pq[2 * i]), max4(pq[2 * i + 1])) == POISON; any |= bad[i]; }
      if (all_lanes(!any)) break;
      MEMB();
      __builtin_amdgcn_s_sleep(PSLEEP);
#pragma unroll
      for (int i = 0; i < 8; ++i)
        if (bad[i]) {
          pq[2 * i] = LD128(rw, lane * 16, WS_PQ + (m32 * 32 + 16 * kh + 2 * i) * 1024);
          pq[2 * i + 1] = LD128(rw, lane * 16, WS_PQ + (m32 * 32 + 16 * kh + 2 * i + 1) * 1024);
        }
    }
    TS();
    ldKV();
#pragma nounroll
    for (unsigned sp = 0; sp < SPIN_CAP; ++sp) {
      unsigned bad[5];
#pragma unroll
      for (int i = 0; i < 4; ++i) bad[i] = bad16(kl[i][0], ta, tb) | bad16(kl[i][1], ta, tb);
      bad[4] = lane < 16 ? (bad16(zl[0], ta, tb) | bad16(zl[1], ta, tb)) : 0u;
      if (all_lanes((((bad[0] | bad[1]) | (bad[2] | bad[3])) | bad[4]) == 0u)) break;
      MEMB();
#pragma unroll
      for (int i = 0; i < 4; ++i)
        if (bad[i]) {
          kl[i][0] = LD128(rw, kvv, kvs + (8 * w + 2 * i) * 1024);
          kl[i][1] = LD128(rw, kvv, kvs + (8 * w + 2 * i) * 1024 + 512);
        }
      if (bad[4]) {
        zl[0] = LD128(rw, zv, kvs + KVZ + w * 512);
        zl[1] = LD128(rw, zv, kvs + KVZ + w * 512 + 32);
      }
    }
    TS();
    {
      const v4u pz = {POISON, POISON, POISON, POISON};
#pragma unroll
      for (int t = 0; t < 6; ++t) ST128(pz, rw, (t < 2 * ntile) ? lane * 16 : OOB, WS_ACT + ((pp * 4 + w) * 86 + 2 * tile0 + t) * 1024);
      MEMB();
    }
    {
      auto cv = [&](v4u x, v4u y) LAM { return v4u{hi16s(x[0], x[1]), hi16s(x[2], x[3]), hi16s(y[0], y[1]), hi16s(y[2], y[3])}; };
#pragma unroll
      for (int i = 0; i < 4; ++i) *(LAS v4u*)(lds + L_X + (8 * w + 2 * i) * 512 + lane * 16) = cv(kl[i][0], kl[i][1]);
      if (lane < 16) *(LAS v4u*)(lds + L_KZ + w * 256 + lane * 16) = cv(zl[0], zl[1]);
    }
    post(lds, mymp, 12);
    wait4(lds, C_LAND, LB0 + 6);
    v4u wf[3][2];
#pragma unroll
    for (int i = 0; i < 2; ++i) { wf[i][0] = tr_frag(ta0, slot_of(0) + i * 2048, 1024); wf[i][1] = tr_frag(ta1, slot_of(0) + i * 2048, 1024); }
    wait1(lds, C_MP + (w ^ 1) * 4, 12);
    v4u kf[16];
    {
      const int kb = (r32 < 16) ? L_X + kh * 8192 + (h2 * 16 + r32) * 16 : L_KZ + kh * 512 + h2 * 16;
      const int ks = (r32 < 16) ? 512 : 32;
#pragma unroll
      for (int i = 0; i < 16; ++i) kf[i] = *(const LAS v4u*)(lds + kb + i * ks);
    }
    post(lds, mymp, 13);
    TS();
    f32x16 acc[2] = {};
    f32x16 o[2];
    v4u of[2];
    const f32x16 zero = {};
    auto scale_lo = [&](const f32x16& oc, float& inv, v4u& dst) LAM {
      inv = __builtin_amdgcn_rcpf(oc[8] + 1e-6f);
      dst[0] = pk2(oc[0] * inv, oc[1] * inv); dst[1] = pk2(oc[2] * inv, oc[3] * inv);
    };
    auto scale_hi = [&](const f32x16& oc, float inv, v4u& dst) LAM {
      dst[2] = pk2(oc[4] * inv, oc[5] * inv); dst[3] = pk2(oc[6] * inv, oc[7] * inv);
    };
    float inv;
    o[0] = MFMA32(kf[0], pq[0], zero);
    o[1] = MFMA32(kf[1], pq[1], zero);
    SB();
    scale_lo(o[0], inv, of[0]); scale_hi(o[0], inv, of[0]);
    SB();
#pragma unroll
    for (int hd = 0; hd < 16; ++hd) {
      acc[0] = MFMA32(wf[hd % 3][0], of[hd & 1], acc[0]);
      if (hd + 1 < 16) scale_lo(o[(hd + 1) & 1], inv, of[(hd + 1) & 1]);
      SB();
      acc[1] = MFMA32(wf[hd % 3][1], of[hd & 1], acc[1]);
      if (hd + 1 < 16) scale_hi(o[(hd + 1) & 1], inv, of[(hd + 1) & 1]);
      SB();
      if (hd + 2 < 16) {
        o[hd & 1] = MFMA32(kf[hd + 2], pq[hd + 2], zero);
        const int sl = slot_of((hd + 2) / 3) + ((hd + 2) % 3) * 2048;
        wf[(hd + 2) % 3][0] = tr_frag(ta0, sl, 1024);
        wf[(hd + 2) % 3][1] = tr_frag(ta1, sl, 1024);
      }
      SB();
    }
    TS();
    wait1(lds, C_MP + (w ^ 1) * 4, 13);
#pragma unroll
    for (int q = 0; q < 4; ++q) xch[(w * 4 + q) * 64 + lane] = f32x4{acc[1][4 * q], acc[1][4 * q + 1], acc[1][4 * q + 2], acc[1][4 * q + 3]};
    post(lds, mymp, 14);
    wait1(lds, C_MP + (w ^ 2) * 4, 14);
    float s1 = 0.f, s2 = 0.f;
#pragma unroll
    for (int q = 0; q < 4; ++q) {
      const f32x4 t = xch[((w ^ 2) * 4 + q) * 64 + lane];
      const unsigned ua = pk2(acc[0][4 * q] + t[0] + bflo(xres[q].x), acc[0][4 * q + 1] + t[1] + bfhi(xres[q].x));
      const unsigned ub = pk2(acc[0][4 * q + 2] + t[2] + bflo(xres[q].y), acc[0][4 * q + 3] + t[3] + bfhi(xres[q].y));
      hres[q >> 1][(q & 1) * 2] = ua;
      hres[q >> 1][(q & 1) * 2 + 1] = ub;
    }
    const int so = WS_H + (m32 * 32 + 4 * mem + 2 * kh) * 1024;
    post(lds, mymp, 15);
    hres[0] = clean4(hres[0]); hres[1] = clean4(hres[1]);
    SB(); WAITV(0);
    ST128(hres[0], rw, lane * 16, so);
    ST128(hres[1], rw, lane * 16, so + 1024);
    SB();
#pragma unroll
    for (int q = 0; q < 8; ++q) {
      const unsigned u = hres[q >> 2][q & 3];
      const float e0 = bflo(u), e1 = bfhi(u);
      s1 += e0 + e1;
      s2 += e0 * e0 + e1 * e1;
    }
    s1 = half_sum(s1); s2 = half_sum(s2);
    const v2u st = {clean(asu(s1)), clean(asu(s2))};
    ST64(st, rw, lane < 32 ? lane * 8 : OOB, WS_HS + m32 * 4096 + (mem * 2 + kh) * 256);
    TS();
  }

  {
    const LB tc = mkbase(ldsb + (4 * h2 + q4) * 64 + trcol);
    const int c32 = pp * 4 + w;
    v4u R[2][6];
    wait4(lds, C_LAND, LB0 + 10);
#pragma unroll
    for (int s = 0; s < 6; ++s) R[0][s] = tr_frag(tc, slot_of(6) + (s % 3) * 4096 + (s / 3) * 1024, 512);
    v4u hf[3][4];
    const int hso = WS_H + c32 * 32 * 1024;
    auto ldH = [&](int g) LAM {
#pragma unroll
      for (int i = 0; i < 4; ++i) hf[g % 3][i] = LD128(rw, lane * 16 + i * 1024, hso + kgC(g) * 4096);
    };
    auto okH = [&](int g) LAM {
      return all_lanes(umax(umax(max4(hf[g % 3][0]), max4(hf[g % 3][1])), umax(max4(hf[g % 3][2]), max4(hf[g % 3][3]))) != POISON);
    };
    __builtin_amdgcn_s_sleep(FIRSTSLEEP);
    ldH(0); ldH(1); ldH(2);
    SB();
    f32x16 acc[3] = {};
#pragma unroll
    for (int j = 0; j < 16; ++j) {
      if ((j & 1) == 0) {
        const int g = j >> 1;
        if (!okH(g)) {
#pragma nounroll
          for (unsigned sp = 0; sp < SPIN_CAP; ++sp) {
            MEMB();
            __builtin_amdgcn_s_sleep(PSLEEP);
            ldH(g);
            if (okH(g)) break;
          }
          if (g + 1 < 8) ldH(g + 1);
          if (g + 2 < 8) ldH(g + 2);
        }
        if (j == 6) wait4(lds, C_LAND, LB0 + 14);
        SB();
        if (j == 0) { post(lds, mymp, 20); TS(); }
      }
#pragma unroll
      for (int s = 0; s < 6; ++s) {
        const int ks = 2 * j + s / 3, tt = s % 3;
        if (j + 1 < 16) R[(j + 1) & 1][s] = tr_frag(tc, slot_of(6 + ((j + 1) >> 1)) + tt * 4096 + (((j + 1) & 1) * 2 + s / 3) * 1024, 512);
        acc[tt] = MFMA32(R[j & 1][s], hf[(ks >> 2) % 3][ks & 3], acc[tt]);
        if (tt == 2 && ks + 12 < 32) hf[(ks >> 2) % 3][ks & 3] = LD128(rw, lane * 16 + (ks & 3) * 1024, hso + kgC((ks >> 2) + 3) * 4096);
        SB();
      }
      if ((j & 3) == 2) post(lds, mymp, 20 + (j >> 1));
    }
    TS();
    wait4(lds, C_MP, 15);
#pragma unroll
    for (int t = 0; t < 3; ++t) {
      xch[(w * 6 + 2 * t) * 64 + lane] = f32x4{acc[t][8], acc[t][9], acc[t][10], acc[t][11]};
      xch[(w * 6 + 2 * t + 1) * 64 + lane] = f32x4{acc[t][12], acc[t][13], acc[t][14], acc[t][15]};
    }
    post(lds, mymp, 40);
    wait4(lds, C_HP, 3);
    {
      const f32x2 st = *(const LAS f32x2*)(lds + L_ST2 + (w * 32 + r32) * 8);
      float o[24];
#pragma unroll
      for (int t = 0; t < 3; ++t)
#pragma unroll
        for (int qq = 0; qq < 2; ++qq) {
          const f32x4 cs = *(const LAS f32x4*)(lds + L_CS1 + (t * 32 + 8 * qq + 4 * h2) * 4);
          const f32x4 bb = *(const LAS f32x4*)(lds + L_B1 + (t * 32 + 8 * qq + 4 * h2) * 4);
#pragma unroll
          for (int i = 0; i < 4; ++i) o[8 * t + 4 * qq + i] = acc[t][4 * qq + i] * st[1] + (st[0] * cs[i] + bb[i]);
        }
#pragma unroll
      for (int t = 0; t < 3; ++t) {
        mishN<8>(o + 8 * t);
        const v4u af = {pk2(o[8 * t], o[8 * t + 1]), pk2(o[8 * t + 2], o[8 * t + 3]), pk2(o[8 * t + 4], o[8 * t + 5]), pk2(o[8 * t + 6], o[8 * t + 7])};
        ST128(clean4(af), rw, (t < ntile) ? lane * 16 : OOB, WS_ACT + (c32 * 86 + 2 * (tile0 + t)) * 1024);
        SB();
      }
    }
    TS();
  }

  {
    v4u R[2][6];
    unsigned landed = wait4(lds, C_LAND, LB0 + 16);
    wait4(lds, C_MP, 40);
#pragma unroll
    for (int s = 0; s < 3; ++s) { R[0][s] = tr_frag(ta0, slot_of(14) + s * 2048, 1024); R[0][3 + s] = tr_frag(ta1, slot_of(14) + s * 2048, 1024); }
    constexpr int NG = 5;
    v4u af[NG][3];
    const int aso = WS_ACT + (m32 * 86 + 43 * kh) * 1024;
    auto ldA = [&](int g) LAM {
#pragma unroll
      for (int i = 0; i < 3; ++i) if (3 * g + i < 43) af[g % NG][i] = LD128(rw, lane * 16, aso + ksD(3 * g + i) * 1024);
    };
    auto okA = [&](int g) LAM {
      unsigned m = max4(af[g % NG][0]);
      if (g < 14) m = umax(m, umax(max4(af[g % NG][1]), max4(af[g % NG][2])));
      return all_lanes(m != POISON);
    };
    __builtin_amdgcn_s_sleep(FIRSTSLEEP);
#pragma unroll
    for (int g = 0; g < NG; ++g) ldA(g);
    SB();
    f32x16 acc[2] = {};
#pragma unroll
    for (int c = 0; c < 15; ++c) {
      if (c + 1 < 15 && landed < (unsigned)(LB0 + 14 + c + 2)) landed = wait4(lds, C_LAND, LB0 + 14 + c + 2);
      if (!okA(c)) {
#pragma nounroll
        for (unsigned sp = 0; sp < SPIN_CAP; ++sp) {
          MEMB();
          __builtin_amdgcn_s_sleep(PSLEEP);
          ldA(c);
          if (okA(c)) break;
        }
#pragma unroll
        for (int g = c + 1; g < c + NG; ++g) if (g < 15) ldA(g);
      }
      SB();
      if (c == 0) TS();
#pragma unroll
      for (int s = 0; s < 3; ++s) {
        const int ks = c * 3 + s;
        if (ks + 3 < 43) {
          R[(c + 1) & 1][s] = tr_frag(ta0, slot_of(14 + c + 1) + s * 2048, 1024);
          R[(c + 1) & 1][3 + s] = tr_frag(ta1, slot_of(14 + c + 1) + s * 2048, 1024);
        }
        if (ks < 43) {
          const v4u a = af[c % NG][s];
          acc[0] = MFMA32(R[c & 1][s], a, acc[0]);
          acc[1] = MFMA32(R[c & 1][3 + s], a, acc[1]);
          if (ks + 3 * NG < 43) af[c % NG][s] = LD128(rw, lane * 16, aso + ksD(ks + 3 * NG) * 1024);
        }
        SB();
      }
      if (c == 3) post(lds, mymp, 54);
    }
    TS();
    wait4(lds, C_HP, 4);
#pragma unroll
    for (int q = 0; q < 4; ++q) xch[(w * 4 + q) * 64 + lane] = f32x4{acc[1][4 * q], acc[1][4 * q + 1], acc[1][4 * q + 2], acc[1][4 * q + 3]};
    post(lds, mymp, 70);
    wait1(lds, C_MP + (w ^ 2) * 4, 70);
    int lz = tid; asm volatile("" : "+v"(lz));
    const rsrc_t rout = __builtin_amdgcn_make_buffer_rsrc((void*)p.out, 0, S_ * D_ * 2, 0x00020000);
    const int oo = (lz & 31) * 1024 + ((lz >> 5) & 1) * 8;
    const int so = (panel * 64 + mg * 32) * 1024 + (mem * 64 + kh * 32) * 2;
#pragma unroll
    for (int q = 0; q < 4; ++q) {
      const f32x4 t = xch[((w ^ 2) * 4 + q) * 64 + lane];
      const unsigned ha = hres[q >> 1][(q & 1) * 2], hb = hres[q >> 1][(q & 1) * 2 + 1];
      v2u o;
      o.x = pk2(acc[0][4 * q] + t[0] + bflo(ha), acc[0][4 * q + 1] + t[1] + bfhi(ha));
      o.y = pk2(acc[0][4 * q + 2] + t[2] + bflo(hb), acc[0][4 * q + 3] + t[3] + bfhi(hb));
      __builtin_amdgcn_raw_buffer_store_b64(o, rout, oo + q * 16, so, OUTAUX);
    }
    TS();
    TSDUMP();
  }
}

extern "C" void kernel_launch(void* const* d_in, const int* in_sizes, int n_in, void* d_out, int out_size, void* d_ws, size_t ws_size,
                              hipStream_t stream) {
  Params p;
  p.x = (const char*)d_in[0]; p.wqkv = (const char*)d_in[1]; p.wo = (const char*)d_in[2];
  p.w1 = (const char*)d_in[4]; p.b1 = (const char*)d_in[5]; p.w2 = (const char*)d_in[6];
  p.out = (char*)d_out;
  unsigned long long nreg = NREG;
  while (nreg > 1 && nreg * (unsigned long long)RSTRIDE > ws_size) nreg >>= 1;
  p.wsr = (unsigned long long)(size_t)d_ws | (nreg - 1);
  (void)hipFuncSetAttribute((const void*)fused_block, hipFuncAttributeMaxDynamicSharedMemorySize, LDS_BYTES);
  fused_block<<<256, 512, LDS_BYTES, stream>>>(p);
}
```

```cpp
#include <hip/hip_runtime.h>
#include <hip/hip_bf16.h>
#include <stdint.h>

#define DI __device__ __forceinline__
#define LAM __attribute__((always_inline))
#define SB() __builtin_amdgcn_sched_barrier(0)
#define MEMB() asm volatile("" ::: "memory")
#define UNLIKELY(x) __builtin_expect(!!(x), 0)
#define LAS __attribute__((address_space(3)))
#define GAS __attribute__((address_space(1)))
typedef unsigned v4u __attribute__((ext_vector_type(4)));
typedef unsigned v2u __attribute__((ext_vector_type(2)));
typedef short s16x4 __attribute__((ext_vector_type(4)));
typedef __bf16 bf16x8 __attribute__((ext_vector_type(8)));
typedef __bf16 bf16x2 __attribute__((ext_vector_type(2)));
typedef float f32x16 __attribute__((ext_vector_type(16)));
typedef float f32x4 __attribute__((ext_vector_type(4)));
typedef float f32x2 __attribute__((ext_vector_type(2)));
typedef unsigned long long u64;
typedef __amdgpu_buffer_rsrc_t rsrc_t;

constexpr int S_ = 2048, D_ = 512, F_ = 1376;
constexpr int WS_EPOCH = 0;
constexpr int WS_XST = 32768;
constexpr int WS_CSQ = 65536;
constexpr int WS_CS1 = 163840;
constexpr int WS_FLAGP = 262144;
constexpr int WS_KV = 327680;
constexpr int KVREP = 36864, KVZ = 32768;
constexpr int WS_HS = 655360;
constexpr int WS_PQ = 1 << 20;
constexpr int WS_H = 3 << 20;
constexpr int WS_ACT = 5 << 20;
constexpr int WS_S1 = WS_ACT + 64 * 86 * 1024;
constexpr int WS_BYTES = WS_S1 + 8 * 34 * 4096;
constexpr int CODE_KB = 50;
constexpr int WS_STAMP = 240 << 20;
constexpr int RSTRIDE = 12 << 20;
constexpr int OOB = 0x40000000;

constexpr int L_ST = 147456;
constexpr int L_RED = 148992;
constexpr int SLOT = 12288, NSLOT = 10;
constexpr int L_X = 122880;
constexpr int L_KZ = L_X + 16384;
constexpr int L_C1 = L_X + 20480;
constexpr int L_ST2 = 159232;
constexpr int L_CS1 = 160256;
constexpr int L_B1 = 161024;
constexpr int L_WARM = 161792;
constexpr int L_CTL = 163584;
constexpr int C_LAND = L_CTL, C_MP = L_CTL + 16, C_HP = L_CTL + 32, C_CNT = L_CTL + 64, C_TAG = L_CTL + 128;
constexpr int LDS_BYTES = 163840;
#define slot_of(c) (((c) % NSLOT) * SLOT)
#define wa_of(c) (65536 + ((c) % 5) * 16384)
constexpr int LB0 = 12;

#define PR(j, st) ((st) * ((j) / 2) + ((j) & 1))
#define ordD0(k) ((k) < 14 ? PR(k, 6) : (k) == 14 ? 42 : (k) < 29 ? 2 + PR((k) - 15, 6) : 4 + PR((k) - 29, 6))
#define ordD1(k) ((k) == 0 ? 0 : (k) < 7 ? 5 + PR((k) - 1, 6) : (k) < 17 ? 23 + PR((k) - 7, 4) : (k) < 25 ? 1 + PR((k) - 17, 6) : (k) < 35 ? 25 + PR((k) - 25, 4) : 3 + PR((k) - 35, 6))
constexpr int NSTG = 14;

#define QSLEEP 18
#define NREG 16
#define GQ0 6
#define GQ1 24
#define DSLEEP 20
#define NH_ 3
#define NG_ 5
#define CHOIST 12
#define HT 2
#define BTRIG 11
#define THIN 3
#define LSPLIT 20
#define RSLOW 8
#define ER 1
#define LATE24 8
#define RDLY 10
#define RSLEEP 1
#define FIRSTSLEEP 14
#define PSLEEP 2
#define LSLEEP 1
constexpr int AUX_SC1 = 16;
constexpr unsigned SPIN_CAP = 1u << 18;
constexpr unsigned LSPIN_CAP = 1u << 21;

#define MFMA32(a, b, c) __builtin_amdgcn_mfma_f32_32x32x16_bf16(__builtin_bit_cast(bf16x8, a), __builtin_bit_cast(bf16x8, b), c, 0, 0, 0)
#define WAITV_(n) asm volatile("s_waitcnt vmcnt(" #n ")" ::: "memory")
#define WAITV(n) WAITV_(n)
#define BAR() do { SB(); asm volatile("s_barrier" ::: "memory"); SB(); } while (0)
#define TH() do { SB(); WAITV(THIN); SB(); } while (0)
#define RLX_AGENT __ATOMIC_RELAXED, __HIP_MEMORY_SCOPE_AGENT
#define LD128(rs, vo, so) __builtin_amdgcn_raw_buffer_load_b128(rs, vo, so, AUX_SC1)
#define LD64(rs, vo, so) __builtin_amdgcn_raw_buffer_load_b64(rs, vo, so, AUX_SC1)
#define ST128(v, rs, vo, so) __builtin_amdgcn_raw_buffer_store_b128(v, rs, (vo) + (so), 0, AUX_SC1)
#define ST64(v, rs, vo, so) __builtin_amdgcn_raw_buffer_store_b64(v, rs, vo, so, AUX_SC1)

DI float asf(unsigned u) { return __builtin_bit_cast(float, u); }
DI unsigned asu(float f) { return __builtin_bit_cast(unsigned, f); }
DI unsigned pk2(float a, float b) {
  f32x2 f = {a, b};
  return __builtin_bit_cast(unsigned, __builtin_convertvector(f, bf16x2));
}
DI float bflo(unsigned u) { return asf(u << 16); }
DI float bfhi(unsigned u) { return asf(u & 0xffff0000u); }
struct LB { unsigned a[3]; };
DI LB mkbase(unsigned a) {
  LB r; r.a[0] = a; r.a[1] = a + 65536u; r.a[2] = a + 131072u;
  asm volatile("" : "+v"(r.a[0]), "+v"(r.a[1]), "+v"(r.a[2]));
  return r;
}
DI const LAS char* lp(const LB& b, int off) { return (const LAS char*)(size_t)b.a[off >> 16] + (off & 65535); }
DI v4u tr_frag(const LB& b, int off, int dt) {
  s16x4 x = __builtin_amdgcn_ds_read_tr16_b64_v4i16((LAS s16x4*)lp(b, off));
  s16x4 y = __builtin_amdgcn_ds_read_tr16_b64_v4i16((LAS s16x4*)lp(b, off + dt));
  v2u ux = __builtin_bit_cast(v2u, x), uy = __builtin_bit_cast(v2u, y);
  return v4u{ux.x, ux.y, uy.x, uy.y};
}
DI float half_sum(float v) {
  auto r = __builtin_amdgcn_permlane32_swap(asu(v), asu(v), false, false);
  const unsigned r0 = r[0], r1 = r[1];
  return asf(r0) + asf(r1);
}
DI float row_pair_sum(float v) {
  auto r = __builtin_amdgcn_permlane16_swap(asu(v), asu(v), false, false);
  const unsigned r0 = r[0], r1 = r[1];
  return asf(r0) + asf(r1);
}
template <int CTRL> DI float dpp_add(float v) { return v + asf(__builtin_amdgcn_update_dpp(0, asu(v), CTRL, 0xf, 0xf, true)); }
DI float wave_sum(float v) {
  v = dpp_add<0xB1>(v); v = dpp_add<0x4E>(v); v = dpp_add<0x141>(v); v = dpp_add<0x140>(v);
  return half_sum(row_pair_sum(v));
}
constexpr unsigned POISON = 0xffffffffu;
DI unsigned umax(unsigned a, unsigned b) { return a > b ? a : b; }
DI unsigned clean(unsigned d) { return d < 0xfffffffeu ? d : 0xfffffffeu; }
DI v4u clean4(v4u v) { return v4u{clean(v.x), clean(v.y), clean(v.z), clean(v.w)}; }
DI unsigned max4(v4u v) { return umax(umax(v.x, v.y), umax(v.z, v.w)); }
DI bool all_lanes(bool ok) { return __builtin_amdgcn_ballot_w64(!ok) == 0ull; }
DI unsigned tagA(unsigned tlo) { return tlo * 0x9E3779B1u; }
DI unsigned tagB(unsigned tlo) { return tlo * 0x85EBCA77u; }
DI v4u tag24(f32x4 v, unsigned t) {
  v4u r;
#pragma unroll
  for (int i = 0; i < 4; ++i) r[i] = ((asu(v[i]) + 0x80u) & 0xffffff00u) | ((t >> (8 * i)) & 0xffu);
  return r;
}
DI unsigned bad24(v4u g, unsigned t) { return ((g.x ^ t) | (g.y ^ (t >> 8)) | (g.z ^ (t >> 16)) | (g.w ^ (t >> 24))) & 0xffu; }
DI f32x4 val24(v4u g) { return f32x4{asf(g.x & 0xffffff00u), asf(g.y & 0xffffff00u), asf(g.z & 0xffffff00u), asf(g.w & 0xffffff00u)}; }
DI v4u tag16(f32x4 v, unsigned ta, unsigned tb) {
  const unsigned p0 = pk2(v[0], v[1]), p1 = pk2(v[2], v[3]);
  return v4u{(p0 << 16) | (ta & 0xffffu), (p0 & 0xffff0000u) | (ta >> 16), (p1 << 16) | (tb & 0xffffu), (p1 & 0xffff0000u) | (tb >> 16)};
}
DI unsigned lo16s(unsigned a, unsigned b) { return __builtin_amdgcn_perm(b, a, 0x05040100u); }
DI unsigned hi16s(unsigned a, unsigned b) { return __builtin_amdgcn_perm(b, a, 0x07060302u); }
DI unsigned bad16(v4u g, unsigned ta, unsigned tb) { return (lo16s(g.x, g.y) ^ ta) | (lo16s(g.z, g.w) ^ tb); }
DI f32x16 rank1(float a, float b, f32x16 c) {
  asm volatile("s_nop 7\n\ts_nop 7" : "+v"(c));
  c = __builtin_amdgcn_mfma_f32_32x32x2f32(a, b, c, 0, 0, 0);
  return c;
}
constexpr float LOG2E = 1.44269504088896341f;
DI f32x2 pkfma(f32x2 a, f32x2 b, f32x2 c) { return __builtin_elementwise_fma(a, b, c); }
DI f32x2 pkmul(f32x2 a, f32x2 b) { return a * b; }
DI f32x2 pkadd(f32x2 a, f32x2 b) { return a + b; }
DI float sfma(float a, float b, float c) { float r; asm("v_fma_f32 %0, %1, %2, %3" : "=v"(r) : "v"(a), "v"(b), "v"(c)); return r; }
DI f32x2 dup(float a) { return f32x2{a, a}; }
#define P2(v, i) (f32x2{(v)[2 * (i)], (v)[2 * (i) + 1]})
#define MFMA_DONE(...) asm volatile("s_nop 7\n\ts_nop 7\n\ts_nop 3" : __VA_ARGS__)
DI unsigned pk2v(f32x2 f) { return __builtin_bit_cast(unsigned, __builtin_convertvector(f, bf16x2)); }
DI void phi8(f32x2 (&te)[8], f32x2 (&tl)[8]) {
  SB();
#pragma unroll
  for (int i = 0; i < 8; ++i) {
    tl[i][0] = __builtin_amdgcn_fmed3f(__builtin_amdgcn_exp2f(tl[i][0]), 0.f, 1.f);
    tl[i][1] = __builtin_amdgcn_fmed3f(__builtin_amdgcn_exp2f(tl[i][1]), 0.f, 1.f);
  }
  SB();
#pragma unroll
  for (int i = 0; i < 8; ++i) { te[i][0] = __builtin_fmaxf(te[i][0], 1e-6f); te[i][1] = __builtin_fmaxf(te[i][1], 1e-6f); }
  SB();
#pragma unroll
  for (int i = 0; i < 8; ++i) te[i] = pkadd(te[i], tl[i]);
}
constexpr float LN2 = 0.693147180559945309f;
DI v4u mish8(const f32x2* a) {
  f32x2 u[4];
  SB();
#pragma unroll
  for (int i = 0; i < 4; ++i) u[i] = f32x2{__builtin_amdgcn_exp2f(a[i][0]), __builtin_amdgcn_exp2f(a[i][1])};
  SB();
#pragma unroll
  for (int i = 0; i < 4; ++i) u[i] = pkadd(u[i], dup(1.f));
#pragma unroll
  for (int i = 0; i < 4; ++i) u[i] = pkfma(u[i], u[i], dup(1.f));
  SB();
#pragma unroll
  for (int i = 0; i < 4; ++i) u[i] = f32x2{__builtin_amdgcn_rcpf(u[i][0]), __builtin_amdgcn_rcpf(u[i][1])};
  SB();
#pragma unroll
  for (int i = 0; i < 4; ++i) u[i] = pkfma(u[i], dup(-2.f * LN2), dup(LN2));
#pragma unroll
  for (int i = 0; i < 4; ++i) u[i] = pkmul(u[i], a[i]);
  return v4u{pk2v(u[0]), pk2v(u[1]), pk2v(u[2]), pk2v(u[3])};
}
DI void glds(rsrc_t rs, int ldsoff, int voff, int soff) {
  asm volatile("s_mov_b32 m0, %0\n\ts_nop 3\n\tbuffer_load_dwordx4 %1, %2, %3 offen lds" ::"s"(ldsoff), "v"(voff), "s"(rs), "s"(soff) : "memory");
}
DI u64 ld_flag(const char* ws, int off) { return __hip_atomic_load((const GAS u64*)(ws + off), RLX_AGENT); }
DI void st_flag(char* ws, int off, u64 v) { __hip_atomic_store((GAS u64*)(ws + off), v, RLX_AGENT); }
DI unsigned min4(v4u v) {
  const unsigned a = v.x < v.y ? v.x : v.y, b = v.z < v.w ? v.z : v.w;
  return __builtin_amdgcn_readfirstlane(a < b ? a : b);
}
DI unsigned wait4(LAS char* lds, int off, unsigned e) {
  SB();
  unsigned m = min4(*(volatile LAS v4u*)(lds + off));
  if (UNLIKELY(m < e)) {
#pragma nounroll
    for (unsigned sp = 0; sp < LSPIN_CAP; ++sp) {
      __builtin_amdgcn_s_sleep(LSLEEP);
      m = min4(*(volatile LAS v4u*)(lds + off));
      if (m >= e) break;
    }
  }
  MEMB(); SB();
  return m;
}
DI void wait1(LAS char* lds, int off, unsigned e) {
  SB();
  if (UNLIKELY(__builtin_amdgcn_readfirstlane(*(volatile LAS unsigned*)(lds + off)) < e)) {
#pragma nounroll
    for (unsigned sp = 0; sp < LSPIN_CAP; ++sp) {
      __builtin_amdgcn_s_sleep(LSLEEP);
      if (__builtin_amdgcn_readfirstlane(*(volatile LAS unsigned*)(lds + off)) >= e) break;
    }
  }
  MEMB(); SB();
}
DI void post(LAS char* lds, int off, unsigned v) {
  SB(); MEMB();
  *(volatile LAS unsigned*)(lds + off) = v;
  MEMB(); SB();
}
DI bool arrive_last(LAS char* lds, int off, unsigned n, int lane) {
  unsigned old = 0;
  if (lane == 0) old = __hip_atomic_fetch_add((LAS unsigned*)(lds + off), 1u, __ATOMIC_RELAXED, __HIP_MEMORY_SCOPE_WORKGROUP);
  return __builtin_amdgcn_readfirstlane(old) == n - 1;
}

#define TS()
#define TSDUMP()

struct Params {
  const char* x; const char* wqkv; const char* wo; const char* w1; const char* b1; const char* w2;
  char* out; u64 wsr;
};

__global__ __launch_bounds__(512, 2) void fused_block(Params p) {
  extern __shared__ __attribute__((aligned(16))) char smem[];
  LAS char* lds = (LAS char*)smem;
  const int tid = threadIdx.x, lane = tid & 63;
  const int w = __builtin_amdgcn_readfirstlane(tid >> 6);
  const int b = blockIdx.x, xcd = b & 7, loc = b >> 3;
  if (tid < 32) *(LAS unsigned*)(lds + L_CTL + tid * 4) = 0u;
  const u64 pc0 = __builtin_amdgcn_s_getpc() & ~255ull;
  auto codepf = [&](int piece) LAM {
    const rsrc_t rc = __builtin_amdgcn_make_buffer_rsrc((void*)pc0, 0, CODE_KB * 1024, 0x00020000);
    glds(rc, L_WARM, lane * 16, piece * 1024);
  };
  codepf((loc & 7) * 8 + w);
  const int panel = (xcd >> 1) * 8 + (loc >> 2), mem = (xcd & 1) * 4 + (loc & 3);
  const int pp = panel >> 1, cm = (panel & 1) * 8 + mem;
  const int ntile = cm < 11 ? 3 : 2, tile0 = min(3 * cm, 2 * cm + 11);
  char* ws = (char*)(p.wsr & ~255ull);
  const int h2 = lane >> 5, r32 = lane & 31;
  const int w4 = w & 3;
  TS();
  const int mg = w & 1, kh = w4 >> 1;
  const int m32 = panel * 2 + mg;
  auto mkrw = [&](unsigned tl) LAM { return __builtin_amdgcn_make_buffer_rsrc((void*)(ws + (size_t)(tl & (unsigned)p.wsr & 255u) * RSTRIDE), 0, WS_BYTES, 0x00020000); };
  const rsrc_t rx = __builtin_amdgcn_make_buffer_rsrc((void*)p.x, 0, S_ * D_ * 2, 0x00020000);
  const rsrc_t rq = __builtin_amdgcn_make_buffer_rsrc((void*)p.wqkv, 0, D_ * 3 * D_ * 2, 0x00020000);
  const rsrc_t r1 = __builtin_amdgcn_make_buffer_rsrc((void*)p.w1, 0, D_ * F_ * 2, 0x00020000);
  const unsigned ldsb = (unsigned)(size_t)lds;
  const int q4 = (lane & 15) >> 2;
  const int trcol = ((lane >> 4) & 1) * 32 + (lane & 3) * 8;
  const int xrow = mg * 32 + r32;
  const int xsw = (xrow >> 1) & 7;
  LAS f32x4* xch = (LAS f32x4*)(lds + L_X);
  const int rot = (2 * mem + panel) & 7;
  auto posA = [&](int t, int half) LAM {
    const int lo = (t == 0 ? 0 : t == 1 ? 15 : 29) + 2 * cm + half;
    const int mid = (t == 0 ? 28 : t == 1 ? 46 : 64) + 2 * cm + half;
    const int hi = (t == 0 ? 28 : 46) + 2 * cm + half;
    return (cm < 7 || (cm == 7 && t == 0 && half == 0)) ? lo : cm < 11 ? mid : hi;
  };
  auto ksD = [&](int k) LAM { return ordD0(k) + kh * (ordD1(k) - ordD0(k)); };

  const int lw = w4;
    const rsrc_t ro = __builtin_amdgcn_make_buffer_rsrc((void*)p.wo, 0, D_ * D_ * 2, 0x00020000);
    const rsrc_t r2 = __builtin_amdgcn_make_buffer_rsrc((void*)p.w2, 0, F_ * D_ * 2, 0x00020000);
    const int wsw = ((lane & 7) ^ (((lane >> 4) & 1) << 2)) * 16;
    const int vo = (lane >> 3) * 1024 + wsw;
    auto mkv1 = [&](int u) LAM {
      const int o = u * 64 + lane, r = (o * 171) >> 11, cc = o - 12 * r;
      return cc < 4 * ntile ? r * 2752 + cc * 16 : OOB;
    };
    const int v1[3] = {mkv1(0), mkv1(1), mkv1(2)};
    auto issueW = [&](int c) LAM {
#pragma unroll
      for (int u = 0; u < 3; ++u) glds(r1, slot_of(c) + lw * 3072 + u * 1024, v1[u], (64 * (c - 6) + 16 * lw) * 2752 + tile0 * 64);
    };
  const int myland = C_LAND + lw * 4;

    auto mkvx = [&](int l) LAM { return (l >> 3) * 1024 + (((l & 7) ^ (((lw & 1) * 4 + (l >> 4)) & 7)) * 16); };
    auto mkvq = [&](int l) LAM { return (l >> 3) * 3072 + ((l & 7) ^ (((l >> 4) & 1) << 2)) * 16; };
    auto issueX = [&](int j0, int vx) LAM {
#pragma unroll
      for (int jj = j0; jj < j0 + 2; ++jj) {
        const int c = jj, kc = (c + rot) & 7;
#pragma unroll
        for (int j = 0; j < 2; ++j) { const int i = lw + 4 * j; glds(rx, c * 8192 + i * 1024, vx, (panel * 64 + 8 * i) * 1024 + kc * 128); }
      }
    };
    auto issueA = [&](int c, int vx, int vq) LAM {
      if (c < 8) {
        const int kc = (c + rot) & 7;
#pragma unroll
        for (int j = 0; j < 2; ++j)
#pragma unroll
          for (int t = 0; t < 2; ++t) {
            const int i = lw + 4 * j;
            glds(rq, wa_of(c) + t * 8192 + i * 1024, vq, (kc * 64 + 8 * i) * 3072 + (t + 1) * 1024 + mem * 128);
          }
      } else {
#pragma unroll
        for (int t = 0; t < 2; ++t) {
          const int kc = (2 * (c - 8) + t + rot) & 7;
#pragma unroll
          for (int j = 0; j < 2; ++j) { const int i = lw + 4 * j; glds(rq, wa_of(c) + t * 8192 + i * 1024, vq, (kc * 64 + 8 * i) * 3072 + mem * 128); }
        }
      }
    };
  const int ms = (3 - (int)((unsigned)(size_t)p.wqkv >> 7)) & 7;
  auto weightpf = [&]() LAM {
    const int gw = b * 4 + w4;
    glds(r1, L_WARM, lane * 16, gw * 1024);
    glds(r1, L_WARM, lane < 22 ? lane * 16 : OOB, 1048576 + gw * 352);
    glds(ro, L_WARM, lane < 32 ? lane * 16 : OOB, gw * 512);
  };
  auto w2pf = [&]() LAM {
    const int gw = b * 4 + w4;
    glds(r2, L_WARM, lane * 16, gw * 1024);
    glds(r2, L_WARM, lane < 22 ? lane * 16 : OOB, 1048576 + gw * 352);
  };
  const int cb = panel >> 3, rb = panel & 7;
  u64 e;
  v4u xr[2], wr[2];
  const int vx0 = mkvx(lane), vq0 = mkvq(lane);
  TS();
  { const char* px = p.x; asm volatile("" :: "s"(px)); }
  asm volatile("s_waitcnt lgkmcnt(0)\n\ts_barrier" ::: "memory");
  TS();
  if (w >= 4) {
    issueX(0, vx0);
    issueX(2, vx0);
  } else {
    e = ld_flag(ws, WS_EPOCH) + 1ull;
#pragma unroll
    for (int i = 0; i < 2; ++i) xr[i] = __builtin_amdgcn_raw_buffer_load_b128(rx, lane * 16, (panel * 64 + 8 * mem + 2 * w + i) * 1024, 0);
    const int vq2 = (cb < 3) ? (lane >> 3) * 3072 + (lane & 7) * 16 : OOB;
#pragma unroll
    for (int i = 0; i < 2; ++i) wr[i] = __builtin_amdgcn_raw_buffer_load_b128(rq, vq2, (64 * rb + 16 * w + 8 * i) * 3072 + cb * 1024 + mem * 128, 0);
  }
  SB(); MEMB();
  TS();
  if (__builtin_expect(w >= 4, 1)) {
    issueX(4, vx0); issueX(6, vx0);
    issueA(0, vx0, vq0); issueA(1, vx0, vq0); issueA(2, vx0, vq0);
    TS();
    issueA(3, vx0, vq0); issueA(4, vx0, vq0);
    WAITV(16); BAR();
    TS();
    WAITV(12); BAR();
  }
  int w2 = w;
  asm volatile("" : "+s"(w2));
  if (w2 >= 4) {
    const int myhp = C_HP + lw * 4;
    int l2 = tid; asm volatile("" : "+v"(l2));
    const int vx = mkvx(l2 & 63), vq = mkvq(l2 & 63);
    v4u stg[NSTG][3];
    auto ldW = [&](int c) LAM {
#pragma unroll
      for (int u = 0; u < 3; ++u) {
        if (c < 6) {
          const int q = lw + 4 * u, k2 = q >= 6 ? 1 : 0, i8 = q - 6 * k2;
          stg[4 + c][u] = __builtin_amdgcn_raw_buffer_load_b128(ro, (c == 5 && i8 >= 2) ? OOB : vo, (k2 * 256 + 48 * c + 8 * i8) * 1024 + mem * 128, 0);
        } else if (c < 14) {
          stg[(c - 10) % NSTG][u] = __builtin_amdgcn_raw_buffer_load_b128(r1, v1[u], (64 * (c - 6) + 16 * lw) * 2752 + tile0 * 64, 0);
        } else if (3 * (c - 14) + u < 43) {
          stg[(c - 10) % NSTG][u] = __builtin_amdgcn_raw_buffer_load_b128(r2, vo, (kh * 688 + 16 * ksD(3 * (c - 14) + u) + 8 * mg) * 1024 + mem * 128, 0);
        }
      }
      MEMB();
    };
    const LB pw1 = mkbase(ldsb + lw * 1024 + lane * 16);
    const LB pw3 = mkbase(ldsb + lw * 3072 + lane * 16);
    const LB pw2 = mkbase(ldsb + kh * 6144 + mg * 1024 + lane * 16);
    auto putW = [&](int c) LAM {
#pragma unroll
      for (int u = 0; u < 3; ++u) {
        if (c < 6) *(LAS v4u*)lp(pw1, slot_of(c) + u * 4096) = stg[4 + c][u];
        else if (c < 14) *(LAS v4u*)lp(pw3, slot_of(c) + u * 1024) = stg[(c - 10) % NSTG][u];
        else if (3 * (c - 14) + u < 43) *(LAS v4u*)lp(pw2, slot_of(c) + u * 2048) = stg[(c - 10) % NSTG][u];
      }
    };
    issueA(5, vx, vq);
    WAITV(12); BAR(); issueA(6, vx, vq);
    WAITV(12); BAR(); issueA(7, vx, vq);
    WAITV(12); BAR(); issueA(8, vx, vq);
    WAITV(12); BAR(); issueA(9, vx, vq);
    WAITV(12); BAR(); issueA(10, vx, vq);
    WAITV(12); BAR();
    u64 tag;
    unsigned tl, th;
    {
      const v2u tg = *(const LAS v2u*)(lds + C_TAG);
      tl = __builtin_amdgcn_readfirstlane(tg.x);
      th = __builtin_amdgcn_readfirstlane(tg.y);
      tag = (u64)tl | ((u64)th << 32);
    }
    const rsrc_t rw = mkrw(tl);
    const bool zred = lw == 2 && (panel & 15) == 5;
    auto poisonE = [&]() LAM {
      const v4u pz = {POISON, POISON, POISON, POISON};
      const int so = (m32 * 32 + 4 * mem + 2 * kh) * 1024;
      ST128(pz, rw, lane * 16, WS_PQ + so);
      ST128(pz, rw, lane * 16, WS_PQ + so + 1024);
      ST128(pz, rw, lane * 16, WS_H + so);
      ST128(pz, rw, lane * 16, WS_H + so + 1024);
      ST128(pz, rw, lane < 16 ? lane * 16 : OOB, WS_HS + m32 * 4096 + (mem * 2 + kh) * 256);
      MEMB(); SB();
    };
    auto flagP = [&]() LAM {
      post(lds, myhp, 1);
      if (arrive_last(lds, C_CNT + 8, 4, lane)) ST64((v2u{tl, th}), rw, lane == 0 ? 0 : OOB, WS_FLAGP + (pp * 16 + cm) * 128);
      MEMB();
    };
    issueA(11, vx, vq);
    poisonE();
    TS();
    BAR();
    weightpf();
    WAITV(3); post(lds, myland, 12); flagP();
    if (!(lw == 3 || zred)) { ldW(0); TH(); ldW(1); TH(); ldW(2); TH(); ldW(3); TH(); ldW(4); TH(); ldW(5); TH(); }
    TS();
    if (lw == 3 || zred) {
      const unsigned ta = tagA(tl), tb = tagB(tl);
      const int rm = mem ^ 1;
      const int so = WS_S1 + (rm * 34 + (zred ? 32 + (panel >> 4) : panel)) * 4096;
      const int dst = WS_KV + (zred ? KVZ + rm * 256 + (panel >> 4) * 128 : rm * 4096 + panel * 128);
      v4u g[4];
      wait4(lds, C_MP, RDLY);
      if (rm == ms) __builtin_amdgcn_s_sleep(RSLOW);
#pragma unroll
      for (int i = 0; i < 4; ++i) g[i] = LD128(rw, lane * 16, so + i * 1024);
#pragma nounroll
      for (unsigned sp = 0; sp < SPIN_CAP; ++sp) {
        unsigned bad[4];
#pragma unroll
        for (int i = 0; i < 4; ++i) bad[i] = bad24(g[i], ta);
        if (all_lanes(((bad[0] | bad[1]) | (bad[2] | bad[3])) == 0u)) break;
        MEMB();
        __builtin_amdgcn_s_sleep(RSLEEP);
#pragma unroll
        for (int i = 0; i < 4; ++i) if (bad[i]) g[i] = LD128(rw, lane * 16, so + i * 1024);
      }
      f32x4 a = (val24(g[0]) + val24(g[1])) + (val24(g[2]) + val24(g[3]));
#pragma unroll
      for (int i = 0; i < 4; ++i) a[i] = half_sum(row_pair_sum(dpp_add<0x128>(a[i])));
      ST128(tag16(a, ta, tb), rw, (lane >> 3) * KVREP + (lane & 7) * 16, dst);
      MEMB();
      ldW(0); ldW(1); ldW(2); ldW(3); ldW(4); ldW(5);
    }
    TS();
    wait4(lds, C_MP, 10);
    putW(0); putW(1); putW(2); putW(3); putW(4); putW(5);
    post(lds, myland, LB0 + 6);
    if (!(lw == 3 || zred)) { ldW(10); TH(); ldW(11); TH(); ldW(12); TH(); ldW(13); TH(); }
    issueW(6); TH(); issueW(7); TH(); issueW(8); TH(); issueW(9);
    TS();
    wait4(lds, C_MP, BTRIG);
    TS();
    {
      v4u cl[4];
      const int vo1 = ((lane & 15) < 6 && lw * 6 + (lane & 15) < ntile * 8) ? (lane >> 4) * 4 * 5632 + (lane & 15) * 16 : OOB;
#pragma unroll
      for (int i = 0; i < 4; ++i) cl[i] = LD128(rw, vo1, WS_CS1 + i * 5632 + tile0 * 128 + lw * 96);
      const rsrc_t rb = __builtin_amdgcn_make_buffer_rsrc((void*)p.b1, 0, F_ * 2, 0x00020000);
      const v2u bb = __builtin_amdgcn_raw_buffer_load_b64(rb, tile0 * 64 + lw * 48 + lane * 8, 0, 0);
      SB(); WAITV(5);
      post(lds, myland, LB0 + 10);
      if (lw == 3 || zred) { ldW(10); ldW(11); ldW(12); ldW(13); }
#pragma unroll
      for (int c = 14; c < LSPLIT; ++c) ldW(c);
      SB();
      const unsigned ta = tagA(tl);
#pragma nounroll
      for (unsigned sp = 0; sp < SPIN_CAP; ++sp) {
        if (all_lanes(vo1 == OOB || ((bad24(cl[0], ta) | bad24(cl[1], ta)) | (bad24(cl[2], ta) | bad24(cl[3], ta))) == 0u)) break;
        MEMB();
        __builtin_amdgcn_s_sleep(PSLEEP);
#pragma unroll
        for (int i = 0; i < 4; ++i) cl[i] = LD128(rw, vo1, WS_CS1 + i * 5632 + tile0 * 128 + lw * 96);
      }
      f32x4 a = (val24(cl[0]) + val24(cl[1])) + (val24(cl[2]) + val24(cl[3]));
#pragma unroll
      for (int i = 0; i < 4; ++i) a[i] = half_sum(row_pair_sum(a[i]));
      if (lane < 6) {
        *(LAS f32x4*)(lds + L_CS1 + lw * 96 + lane * 16) = a;
        *(LAS f32x4*)(lds + L_B1 + lw * 96 + lane * 16) = f32x4{bflo(bb.x) * LOG2E, bfhi(bb.x) * LOG2E, bflo(bb.y) * LOG2E, bfhi(bb.y) * LOG2E};
      }
      post(lds, myhp, 2);
    }
    wait4(lds, C_MP, 14);
    if (b == 0 && lw == 0 && lane == 0) st_flag(ws, WS_EPOCH, tag);
    putW(10); putW(11); putW(12); putW(13);
    post(lds, myland, LB0 + 14);
    TS();
    putW(14); putW(15);
    post(lds, myland, LB0 + 16);
    TS();
    {
      const int c32 = pp * 4 + lw;
      v2u hs[8];
      wait1(lds, C_MP + lw * 4, 20);
#pragma nounroll
      for (unsigned sp = 0; sp < SPIN_CAP; ++sp) {
        unsigned m = 0;
#pragma unroll
        for (int i = 0; i < 8; ++i) hs[i] = LD64(rw, (8 * h2 + i) * 256 + r32 * 8, WS_HS + c32 * 4096);
#pragma unroll
        for (int i = 0; i < 8; ++i) m = umax(m, umax(hs[i].x, hs[i].y));
        if (all_lanes(m != POISON)) break;
        MEMB();
        __builtin_amdgcn_s_sleep(PSLEEP);
      }
      float s1 = 0.f, s2 = 0.f;
#pragma unroll
      for (int i = 0; i < 8; ++i) { s1 += asf(hs[i].x); s2 += asf(hs[i].y); }
      s1 = half_sum(s1); s2 = half_sum(s2);
      const float mu = s1 * (1.f / 512.f);
      const float rstd = __builtin_amdgcn_rsqf(__builtin_fmaxf(s2 * (1.f / 512.f) - mu * mu, 0.f) + 1e-5f) * LOG2E;
      const float nm = -mu * rstd;
      if (lane < 32) *(LAS f32x2*)(lds + L_ST2 + (lw * 32 + r32) * 8) = f32x2{nm, rstd};
      post(lds, myhp, 3);
      wait4(lds, C_HP, 2);
      f32x2 o[4 * HT];
#pragma unroll
      for (int t = 0; t < HT; ++t)
#pragma unroll
        for (int qq = 0; qq < 2; ++qq) {
          const f32x4 cs = *(const LAS f32x4*)(lds + L_CS1 + (t * 32 + 16 + 8 * qq + 4 * h2) * 4);
          const f32x4 bb = *(const LAS f32x4*)(lds + L_B1 + (t * 32 + 16 + 8 * qq + 4 * h2) * 4);
#pragma unroll
          for (int i = 0; i < 2; ++i) o[4 * t + 2 * qq + i] = f32x2{sfma(nm, cs[2 * i], bb[2 * i]), sfma(nm, cs[2 * i + 1], bb[2 * i + 1])};
        }
#pragma unroll
      for (int i = 0; i < 4 * HT; ++i) asm volatile("" : "+v"(o[i]));
      wait1(lds, C_MP + lw * 4, 30);
#pragma unroll
      for (int c = LSPLIT; c < 29; ++c) ldW(c);
      TS();
      wait1(lds, C_MP + lw * 4, 40);
      f32x4 pv[2 * HT];
#pragma unroll
      for (int t = 0; t < 2 * HT; ++t) pv[t] = xch[(lw * 6 + t) * 64 + lane];
      post(lds, myhp, 4);
      SB();
#pragma unroll
      for (int t = 0; t < HT; ++t) {
#pragma unroll
        for (int i = 0; i < 4; ++i) o[4 * t + i] = pkfma(P2(pv[2 * t + (i >> 1)], i & 1), dup(rstd), o[4 * t + i]);
        const v4u af = mish8(o + 4 * t);
        ST128(clean4(af), rw, (t < ntile) ? lane * 16 : OOB, WS_ACT + (c32 * 86 + posA(t, 1)) * 1024);
        SB();
      }
    }
    TS();
    wait4(lds, C_MP, 40);
    putW(16); putW(17); putW(18); putW(19); post(lds, myland, LB0 + 20);
    putW(20); putW(21); putW(22); putW(23); post(lds, myland, LB0 + 24);
    TS();
    wait4(lds, C_MP, 52);
    putW(24); putW(25); putW(26); post(lds, myland, LB0 + 27);
    wait4(lds, C_MP, 54);
    putW(27); putW(28); post(lds, myland, LB0 + 29);
    TS();
    TSDUMP();
    return;
  }

  const int mymp = C_MP + w * 4;
  v2u xres[4];
  unsigned xs[4];
#pragma unroll
  for (int s = 0; s < 4; ++s) {
    xs[s] = ldsb + xrow * 128 + (((2 * s + h2) ^ xsw) * 16);
    asm volatile("" : "+v"(xs[s]));
  }
  unsigned w1r[8];
  const LB tn = mkbase(ldsb + (8 * h2 + q4) * 128 + ((kh ^ (q4 >> 1)) * 64) + trcol);
  const unsigned tlo = __builtin_amdgcn_readfirstlane((unsigned)e), thi = __builtin_amdgcn_readfirstlane((unsigned)(e >> 32));
  const rsrc_t rw = mkrw(tlo);
  {
    if (tid == 0) *(LAS v2u*)(lds + C_TAG) = v2u{tlo, thi};
    TS();
    float mean[2], var[2];
    float ee[2][8];
#pragma unroll
    for (int i = 0; i < 2; ++i) {
#pragma unroll
      for (int j = 0; j < 4; ++j) { ee[i][2 * j] = bflo(xr[i][j]); ee[i][2 * j + 1] = bfhi(xr[i][j]); }
      mean[i] = ((ee[i][0] + ee[i][1]) + (ee[i][2] + ee[i][3])) + ((ee[i][4] + ee[i][5]) + (ee[i][6] + ee[i][7]));
    }
#pragma unroll
    for (int i = 0; i < 2; ++i) mean[i] = wave_sum(mean[i]) * (1.f / 512.f);
#pragma unroll
    for (int i = 0; i < 2; ++i) {
      float v = 0.f;
#pragma unroll
      for (int j = 0; j < 8; ++j) { const float d = ee[i][j] - mean[i]; v += d * d; }
      var[i] = v;
    }
#pragma unroll
    for (int i = 0; i < 2; ++i) var[i] = wave_sum(var[i]) * (1.f / 512.f);
    {
      const bool odd = lane & 1;
      const v4u st = {asu(odd ? mean[1] : mean[0]), tlo, asu(__builtin_amdgcn_rsqf((odd ? var[1] : var[0]) + 1e-5f)), thi};
      ST128(st, rw, lane < 2 ? lane * 16 : OOB, WS_XST + (panel * 64 + 8 * mem + 2 * w) * 16);
    }
    TS();
    {
      float a[8];
#pragma unroll
      for (int j = 0; j < 4; ++j) { a[2 * j] = bflo(wr[0][j]) + bflo(wr[1][j]); a[2 * j + 1] = bfhi(wr[0][j]) + bfhi(wr[1][j]); }
#pragma unroll
      for (int j = 0; j < 8; ++j) a[j] = half_sum(row_pair_sum(dpp_add<0x128>(a[j])));
      if (lane < 8) {
        LAS f32x4* t = (LAS f32x4*)(lds + L_RED + 4096 + w * 256 + lane * 32);
        t[0] = f32x4{a[0], a[1], a[2], a[3]}; t[1] = f32x4{a[4], a[5], a[6], a[7]};
      }
    }
    MEMB();
    if (arrive_last(lds, C_CNT + 4, 4, lane)) {
      MEMB();
      const LAS f32x2* t2 = (const LAS f32x2*)(lds + L_RED + 4096) + r32;
      const f32x2 a = (t2[0] + t2[32]) + (t2[64] + t2[96]);
      const v4u o = {asu(a[0]), tlo, asu(a[1]), thi};
      ST128(o, rw, (lane < 32 && cb < 3) ? lane * 16 : OOB, WS_CSQ + ((mem * 3 + cb) * 8 + rb) * 512);
    }
    MEMB();
    TS();
  }
  TS();

  {
    f32x16 ak = {}, av = {};
    f32x4 rr[4], rl[4], nn[4];
    float csk = 0.f, csv = 0.f;
    v4u gcs[4];
    const int gco = w < 3 ? WS_CSQ + (mem * 3 + w) * 4096 : WS_XST + panel * 1024;
    const int gcv = w < 3 ? h2 * 2048 + r32 * 16 : lane * 16, gstr = w < 3 ? 512 : 0;
    auto ldG = [&]() LAM {
#pragma unroll
      for (int i = 0; i < 4; ++i) gcs[i] = LD128(rw, gcv, gco + i * gstr);
    };
    auto badG = [&]() LAM {
      return (((gcs[0][1] ^ tlo) | (gcs[0][3] ^ thi)) | ((gcs[1][1] ^ tlo) | (gcs[1][3] ^ thi))) | (((gcs[2][1] ^ tlo) | (gcs[2][3] ^ thi)) | ((gcs[3][1] ^ tlo) | (gcs[3][3] ^ thi)));
    };
    unsigned gbad = 0;
    float ga0 = 0.f, ga1 = 0.f;
    const unsigned gad = ldsb + (w == 3 ? L_ST + lane * 4 : L_ST + 512 + (w & 1) * 128 + (w >> 1) * 512 + r32 * 4);
    v4u xf[5], wk[5], wv[5];
    auto rdx = [&](int s) LAM { return *(const LAS v4u*)((const LAS char*)(size_t)xs[s & 3] + (s >> 2) * 8192); };
    BAR();
    TS();
#pragma unroll
    for (int s = 0; s < 4; ++s) {
      xf[s] = rdx(s);
      wk[s] = tr_frag(tn, wa_of(0) + s * 2048, 512);
      wv[s] = tr_frag(tn, wa_of(0) + 8192 + s * 2048, 512);
    }
#pragma unroll
    for (int s = 0; s < 32; ++s) {
      const int n = s + 4;
      if ((s & 3) == 0) {
        asm volatile("s_waitcnt lgkmcnt(0)" ::: "memory");
        BAR();
      }
      const v4u xc = xf[s % 5];
      const bool last = (s & 3) >= 4 - ER;
      if (last && n < 32) {
        xf[n % 5] = rdx(n); wk[n % 5] = tr_frag(tn, wa_of(n >> 2) + (n & 3) * 2048, 512);
        wv[n % 5] = tr_frag(tn, wa_of(n >> 2) + 8192 + (n & 3) * 2048, 512);
        SB();
      }
      ak = MFMA32(xc, wk[s % 5], ak);
      if (!last && n < 32) { xf[n % 5] = rdx(n); wk[n % 5] = tr_frag(tn, wa_of(n >> 2) + (n & 3) * 2048, 512); }
      SB();
      av = MFMA32(xc, wv[s % 5], av);
      if (!last && n < 32) wv[n % 5] = tr_frag(tn, wa_of(n >> 2) + 8192 + (n & 3) * 2048, 512);
      SB();
      if (s == GQ0) ldG();
      if (s == GQ1 - 2) { gbad = badG(); asm volatile("" : "+v"(gbad)); SB(); }
      if (s == GQ1 - 1) {
        if (UNLIKELY(!all_lanes(gbad == 0u))) {
#pragma nounroll
          for (unsigned sp = 0; sp < SPIN_CAP; ++sp) {
            MEMB();
            ldG();
            if (all_lanes(badG() == 0u)) break;
          }
        }
        SB();
        ga0 = (asf(gcs[0][0]) + asf(gcs[1][0])) + (asf(gcs[2][0]) + asf(gcs[3][0]));
        ga1 = (asf(gcs[0][2]) + asf(gcs[1][2])) + (asf(gcs[2][2]) + asf(gcs[3][2]));
        asm volatile("" : "+v"(ga0), "+v"(ga1));
        SB();
      }
      if (s == GQ1) {
        ga0 = half_sum(ga0); ga1 = half_sum(ga1);
        unsigned st = w == 3 ? ~0u : 0u;
        asm volatile("" : "+v"(st));
        const unsigned u0 = ((gcs[0][0] ^ 0x80000000u) & st) | (asu(ga0) & ~st), u1 = (gcs[0][2] & st) | (asu(ga1) & ~st);
        *(LAS unsigned*)(size_t)gad = u0;
        *(LAS unsigned*)(size_t)(gad + 256u) = u1;
        SB();
      }
      if (s == 28) {
        const LAS float* cp = (const LAS float*)(lds + L_ST + 512 + (r32 & 1) * 256 + (kh * 16 + (r32 >> 1)) * 4);
        csk = cp[32]; csv = cp[128];
#pragma unroll
        for (int q = 0; q < 4; ++q) {
          const LAS f32x4* sp = (const LAS f32x4*)(lds + L_ST + (mg * 32 + 8 * q + 4 * h2) * 4);
          nn[q] = sp[0]; rr[q] = sp[16];
        }
      }
      if (s == 30) {
#pragma unroll
        for (int q = 0; q < 4; ++q) {
#pragma unroll
          for (int i = 0; i < 4; ++i) rl[q][i] = sfma(rr[q][i], LOG2E, 0.f);
        }
        SB();
      }
    }
    TS();
    f32x2 kk[8], kl2[8];
#pragma unroll
    for (int i = 0; i < 8; ++i) kk[i] = pkfma(P2(nn[i >> 1], i & 1), dup(csk), P2(ak, i));
#pragma unroll
    for (int i = 0; i < 8; ++i) { kl2[i] = pkmul(kk[i], P2(rl[i >> 1], i & 1)); kk[i] = pkfma(kk[i], P2(rr[i >> 1], i & 1), dup(1e-6f)); }
    phi8(kk, kl2);
    v4u pkf[2], vf[2];
#pragma unroll
    for (int i = 0; i < 8; ++i) kl2[i] = pkfma(P2(nn[i >> 1], i & 1), dup(csv), P2(av, i));
#pragma unroll
    for (int i = 0; i < 8; ++i) kl2[i] = pkmul(kl2[i], P2(rr[i >> 1], i & 1));
#pragma unroll
    for (int i = 0; i < 8; ++i) { pkf[i >> 2][i & 3] = pk2v(kk[i]); vf[i >> 2][i & 3] = pk2v(kl2[i]); }
    f32x16 kv = {};
    kv = MFMA32(pkf[0], vf[0], kv);
    kv = MFMA32(pkf[1], vf[1], kv);
    const float zs = (((kk[0][0] + kk[0][1]) + (kk[1][0] + kk[1][1])) + ((kk[2][0] + kk[2][1]) + (kk[3][0] + kk[3][1]))) +
                     (((kk[4][0] + kk[4][1]) + (kk[5][0] + kk[5][1])) + ((kk[6][0] + kk[6][1]) + (kk[7][0] + kk[7][1])));
    LAS float* red = (LAS float*)(lds + L_RED);
    {
      const bool hi = (lane & 16) != 0;
      float lo8[8], hi8[8];
#pragma unroll
      for (int i = 0; i < 8; ++i) { lo8[i] = kv[i]; hi8[i] = kv[8 + i]; asm volatile("" : "+v"(lo8[i]), "+v"(hi8[i])); }
      LAS float* dst = red + mg * 1024 + kh * 512 + (hi ? 256 : 0) + h2 * 64 + (lane & 15) * 4;
#pragma unroll
      for (int pp = 0; pp < 2; ++pp) {
        f32x4 v;
#pragma unroll
        for (int i = 0; i < 4; ++i) v[i] = hi ? hi8[4 * pp + i] : lo8[4 * pp + i];
        *(LAS f32x4*)(dst + pp * 128) = v;
      }
      red[2048 + (mg * 2 + h2) * 64 + kh * 32 + r32] = zs;
    }
    post(lds, mymp, 9);
    wait1(lds, C_MP + (w ^ 1) * 4, 9);
    {
      const unsigned ta = tagA(tlo);
      const int hl = 2 * kh + mg;
      const f32x4 v = *(const LAS f32x4*)(red + hl * 256 + lane * 4) + *(const LAS f32x4*)(red + 1024 + hl * 256 + lane * 4);
      const LAS f32x4* zp = (const LAS f32x4*)(red + 2048 + kh * 32 + (lane & 7) * 4);
      const f32x4 zv = (zp[0] + zp[16]) + (zp[32] + zp[48]);
      ST128(tag24(v, ta), rw, (lane >> 3) * 4096 + (lane & 7) * 16, WS_S1 + (mem * 34 + hl * 8) * 4096 + panel * 128);
      if (mg == 0) ST128(tag24(zv, ta), rw, lane < 8 ? lane * 16 : OOB, WS_S1 + (mem * 34 + 32 + kh) * 4096 + panel * 128);
      MEMB();
    }
#pragma unroll
    for (int i = 0; i < 8; ++i) w1r[i] = __builtin_amdgcn_raw_buffer_load_b32(r1, lane < ntile * 16 ? lane * 4 : OOB, (32 * pp + 8 * w + i) * 2752 + tile0 * 64, 0);
    TS();
    f32x16 aq = {}, aq2 = {};
    {
#pragma unroll
      for (int q = 0; q < 4; ++q) xres[q] = *(const LAS v2u*)(lds + ((mem - rot) & 7) * 8192 + xrow * 128 + (((kh * 4 + q) ^ xsw) * 16) + h2 * 8);
      wait4(lds, C_LAND, 12);
      constexpr int QD = 6;
      v4u xq[QD + 1], wq[QD + 1];
      auto rdq = [&](int s) LAM { xq[s % (QD + 1)] = rdx(s); wq[s % (QD + 1)] = tr_frag(tn, wa_of(8 + (s >> 3)) + (s & 7) * 2048, 512); };
#pragma unroll
      for (int s = 0; s < QD; ++s) rdq(s);
      SB();
#pragma unroll
      for (int s = 0; s < 32; ++s) {
        if (s & 1) aq2 = MFMA32(wq[s % (QD + 1)], xq[s % (QD + 1)], aq2);
        else aq = MFMA32(wq[s % (QD + 1)], xq[s % (QD + 1)], aq);
        if (s + QD < 32) rdq(s + QD);
        if (s + QD == 31) post(lds, mymp, 10);
        SB();
      }
    }
    const LAS float* stp = (const LAS float*)(lds + L_ST) + xrow;
    const float st0 = stp[0], st1 = stp[64], st2 = st1 * LOG2E;
    const float csq = *(const LAS float*)(lds + L_ST + 512 + (r32 & 1) * 256 + (kh * 16 + (r32 >> 1)) * 4);
    aq = rank1(h2 ? 0.f : csq, st0, aq);
    f32x2 t[8], tl2[8];
#pragma unroll
    for (int i = 0; i < 8; ++i) t[i] = pkadd(P2(aq, i), P2(aq2, i));
#pragma unroll
    for (int i = 0; i < 8; ++i) { tl2[i] = pkmul(t[i], dup(st2)); t[i] = pkfma(t[i], dup(st1), dup(1e-6f)); }
    phi8(t, tl2);
    const v4u f0 = {pk2v(t[0]), pk2v(t[1]), pk2v(t[2]), pk2v(t[3])};
    const v4u f1 = {pk2v(t[4]), pk2v(t[5]), pk2v(t[6]), pk2v(t[7])};
    SB();
    TS();
    const int so = WS_PQ + (m32 * 32 + 4 * mem + 2 * kh) * 1024;
    wait1(lds, C_HP + w * 4, 1);
    ST128(clean4(f0), rw, lane * 16, so);
    ST128(clean4(f1), rw, lane * 16, so + 1024);
    MEMB();
    v2u fp = LD64(rw, (lane & 15) * 128, WS_FLAGP + pp * 2048);
    if (UNLIKELY(!all_lanes(fp.x == tlo && fp.y == thi))) {
#pragma nounroll
      for (unsigned sp = 0; sp < SPIN_CAP; ++sp) {
        MEMB();
        __builtin_amdgcn_s_sleep(PSLEEP);
        fp = LD64(rw, (lane & 15) * 128, WS_FLAGP + pp * 2048);
        if (all_lanes(fp.x == tlo && fp.y == thi)) break;
      }
    }
    MEMB();
    TS();
  }

  const LB ta0 = mkbase(ldsb + (4 * h2 + q4) * 128 + ((q4 >> 1) ^ kh) * 64 + trcol + kh * 6144);
  const LB ta1 = mkbase(ldsb + (4 * h2 + q4) * 128 + ((q4 >> 1) ^ kh ^ 1) * 64 + trcol + kh * 6144);
  v4u hres[2];
  {
    v4u kl[4][2], zl[2];
    const int kvv = h2 * 1024 + r32 * 16, kvs = WS_KV + xcd * KVREP + (16 * kh + 2 * mg) * 1024;
    const int zv = lane < 16 ? (lane >> 2) * 256 + ((lane >> 1) & 1) * 64 + (lane & 1) * 16 : OOB;
    const int zs0 = WS_KV + xcd * KVREP + KVZ + (16 * kh + 2 * mg) * 64;
    auto ldKV = [&]() LAM {
#pragma unroll
      for (int i = 0; i < 4; ++i) {
        kl[i][0] = LD128(rw, kvv, kvs + i * 4096);
        kl[i][1] = LD128(rw, kvv, kvs + i * 4096 + 512);
      }
      zl[0] = LD128(rw, zv, zs0);
      zl[1] = LD128(rw, zv, zs0 + 32);
    };
    const unsigned ta = tagA(tlo), tb = tagB(tlo);
    auto poisonA = [&]() LAM {
      const v4u pz = {POISON, POISON, POISON, POISON};
#pragma unroll
      for (int t = 0; t < 6; ++t) ST128(pz, rw, (t < 2 * ntile) ? lane * 16 : OOB, WS_ACT + ((pp * 4 + w) * 86 + posA(t >> 1, t & 1)) * 1024);
      MEMB();
    };
    w2pf();
    __builtin_amdgcn_s_sleep(QSLEEP);
    TS();
    v4u pq[16];
#pragma unroll
    for (int i = 0; i < 16; ++i) pq[i] = LD128(rw, lane * 16, WS_PQ + (m32 * 32 + 16 * kh + i) * 1024);
    SB();
    TS();
    {
      float a0 = 0.f, a1 = 0.f;
#pragma unroll
      for (int i = 0; i < 8; ++i) { a0 += bflo(w1r[i]); a1 += bfhi(w1r[i]); }
      wait4(lds, C_MP, 10);
      ((LAS f32x2*)(lds + L_C1))[w * 64 + lane] = f32x2{a0, a1};
      MEMB();
      if (arrive_last(lds, C_CNT, 4, lane)) {
        MEMB();
        const LAS f32x4* t = (const LAS f32x4*)(lds + L_C1) + (lane & 31);
        ST128(tag24((t[0] + t[32]) + (t[64] + t[96]), ta), rw, (lane < ntile * 8) ? lane * 16 : OOB, WS_CS1 + pp * 5632 + tile0 * 128);
      }
      MEMB();
    }
    unsigned pqm = 0;
#pragma unroll
    for (int i = 0; i < 16; ++i) pqm = umax(pqm, max4(pq[i]));
    if (UNLIKELY(!all_lanes(pqm != POISON)))
#pragma nounroll
    for (unsigned sp = 0; sp < SPIN_CAP; ++sp) {
      bool bad[8], any = false;
#pragma unroll
      for (int i = 0; i < 8; ++i) { bad[i] = umax(max4(pq[2 * i]), max4(pq[2 * i + 1])) == POISON; any |= bad[i]; }
      if (all_lanes(!any)) break;
      MEMB();
      __builtin_amdgcn_s_sleep(PSLEEP);
#pragma unroll
      for (int i = 0; i < 8; ++i)
        if (bad[i]) {
          pq[2 * i] = LD128(rw, lane * 16, WS_PQ + (m32 * 32 + 16 * kh + 2 * i) * 1024);
          pq[2 * i + 1] = LD128(rw, lane * 16, WS_PQ + (m32 * 32 + 16 * kh + 2 * i + 1) * 1024);
        }
    }
    TS();
    ldKV();
    bool late = false;
    auto pollKV = [&](bool first) LAM {
#pragma nounroll
      for (unsigned sp = 0; sp < SPIN_CAP; ++sp) {
        unsigned bad[5];
#pragma unroll
        for (int i = first ? 0 : 3; i < 4; ++i) bad[i] = bad16(kl[i][0], ta, tb) | bad16(kl[i][1], ta, tb);
        bad[4] = lane < 16 ? (bad16(zl[0], ta, tb) | bad16(zl[1], ta, tb)) : 0u;
        const bool okb = all_lanes((bad[3] | bad[4]) == 0u);
        const bool oka = first ? all_lanes((((bad[0] | bad[1]) | bad[2]) | (lane < 12 ? bad[4] : 0u)) == 0u) : okb;
        if (oka && okb) break;
        MEMB();
#pragma unroll
        for (int i = first ? 0 : 3; i < 4; ++i)
          if (bad[i]) {
            kl[i][0] = LD128(rw, kvv, kvs + i * 4096);
            kl[i][1] = LD128(rw, kvv, kvs + i * 4096 + 512);
          }
        if (first && oka) { late = true; break; }
        if (bad[4]) {
          zl[0] = LD128(rw, zv, zs0);
          zl[1] = LD128(rw, zv, zs0 + 32);
        }
      }
    };
    auto cv = [&](v4u x, v4u y) LAM { return v4u{hi16s(x[0], x[1]), hi16s(x[2], x[3]), hi16s(y[0], y[1]), hi16s(y[2], y[3])}; };
    const int kw = L_X + (16 * kh + 2 * mg) * 512 + lane * 16, zw = L_KZ + (16 * kh + 2 * mg) * 32 + (lane >> 2) * 128 + (lane & 3) * 16;
    f32x16 acc[2] = {};
#pragma unroll
    for (int q = 0; q < 4; ++q) {
      acc[0][4 * q] = bflo(xres[q].x); acc[0][4 * q + 1] = bfhi(xres[q].x); acc[0][4 * q + 2] = bflo(xres[q].y); acc[0][4 * q + 3] = bfhi(xres[q].y);
    }
    asm volatile("" : "+v"(acc[0]));
    wait4(lds, C_LAND, LB0 + 6);
    v4u wf[3][2];
#pragma unroll
    for (int i = 0; i < 2; ++i) { wf[i][0] = tr_frag(ta0, slot_of(0) + i * 2048, 1024); wf[i][1] = tr_frag(ta1, slot_of(0) + i * 2048, 1024); }
    {
      unsigned bad = lane < 16 ? (bad16(zl[0], ta, tb) | bad16(zl[1], ta, tb)) : 0u;
#pragma unroll
      for (int i = 0; i < 4; ++i) bad |= bad16(kl[i][0], ta, tb) | bad16(kl[i][1], ta, tb);
      if (UNLIKELY(!all_lanes(bad == 0u))) pollKV(true);
    }
    TS();
#pragma unroll
    for (int i = 0; i < 3; ++i) *(LAS v4u*)(lds + kw + i * 2048) = cv(kl[i][0], kl[i][1]);
    if (lane < 16) *(LAS v4u*)(lds + zw) = cv(zl[0], zl[1]);
    if (!late) *(LAS v4u*)(lds + kw + 3 * 2048) = cv(kl[3][0], kl[3][1]);
    post(lds, mymp, late ? 11 : 12);
    poisonA();
    wait1(lds, C_MP + (w ^ 1) * 4, 11);
    v4u kf[16];
    const int kb = (r32 < 16) ? L_X + kh * 8192 + (h2 * 16 + r32) * 16 : L_KZ + kh * 512 + h2 * 16;
    const int ks = (r32 < 16) ? 512 : 32;
#pragma unroll
    for (int i = 0; i < 12; ++i) kf[i] = *(const LAS v4u*)(lds + kb + i * ks);
    TS();
    f32x16 o[2];
    v4u of[2];
    const f32x16 zero = {};
    auto scale_lo = [&](const f32x16& oc, float inv, v4u& dst) LAM {
      float a, b, c, d;
      asm("v_mul_f32 %0, %4, %8\n\tv_mul_f32 %1, %5, %8\n\tv_mul_f32 %2, %6, %8\n\tv_mul_f32 %3, %7, %8\n\tv_cvt_pk_bf16_f32 %0, %0, %1\n\tv_cvt_pk_bf16_f32 %2, %2, %3"
          : "=&v"(a), "=&v"(b), "=&v"(c), "=&v"(d) : "v"(oc[0]), "v"(oc[1]), "v"(oc[2]), "v"(oc[3]), "v"(inv));
      dst[0] = asu(a); dst[1] = asu(c);
    };
    auto scale_hi = [&](const f32x16& oc, float inv, v4u& dst) LAM {
      float a, b, c, d;
      asm("v_mul_f32 %0, %4, %8\n\tv_mul_f32 %1, %5, %8\n\tv_mul_f32 %2, %6, %8\n\tv_mul_f32 %3, %7, %8\n\tv_cvt_pk_bf16_f32 %0, %0, %1\n\tv_cvt_pk_bf16_f32 %2, %2, %3"
          : "=&v"(a), "=&v"(b), "=&v"(c), "=&v"(d) : "v"(oc[4]), "v"(oc[5]), "v"(oc[6]), "v"(oc[7]), "v"(inv));
      dst[2] = asu(a); dst[3] = asu(c);
    };
    float inv;
    o[0] = MFMA32(kf[0], pq[0], zero);
    o[1] = MFMA32(kf[1], pq[1], zero);
    SB();
    inv = __builtin_amdgcn_rcpf(o[0][8] + 1e-6f);
    MFMA_DONE("+v"(inv));
    scale_lo(o[0], inv, of[0]); scale_hi(o[0], inv, of[0]);
    SB();
#pragma unroll
    for (int hd = 0; hd < 16; ++hd) {
      if (hd == 8) {
        if (UNLIKELY(late)) {
          pollKV(false);
          *(LAS v4u*)(lds + kw + 3 * 2048) = cv(kl[3][0], kl[3][1]);
          if (lane < 16) *(LAS v4u*)(lds + zw) = cv(zl[0], zl[1]);
          post(lds, mymp, 12);
        }
        wait1(lds, C_MP + (w ^ 1) * 4, 12);
#pragma unroll
        for (int i = 12; i < 16; ++i) kf[i] = *(const LAS v4u*)(lds + kb + i * ks);
        post(lds, mymp, 13);
      }
      if (hd + 2 < 16) {
        o[hd & 1] = MFMA32(kf[hd + 2], pq[hd + 2], zero);
        SB();
        const int sl = slot_of((hd + 2) / 3) + ((hd + 2) % 3) * 2048;
        wf[(hd + 2) % 3][0] = tr_frag(ta0, sl, 1024);
        wf[(hd + 2) % 3][1] = tr_frag(ta1, sl, 1024);
      }
      if (hd + 1 < 16) inv = __builtin_amdgcn_rcpf(o[(hd + 1) & 1][8] + 1e-6f);
      else asm volatile("s_nop 1");
      SB();
      acc[0] = MFMA32(wf[hd % 3][0], of[hd & 1], acc[0]);
      SB();
      if (hd + 1 < 16) scale_lo(o[(hd + 1) & 1], inv, of[(hd + 1) & 1]);
      SB();
      acc[1] = MFMA32(wf[hd % 3][1], of[hd & 1], acc[1]);
      SB();
      if (hd + 1 < 16) scale_hi(o[(hd + 1) & 1], inv, of[(hd + 1) & 1]);
      if (hd + 1 < 16) asm volatile("" :: "v"(o[(hd + 1) & 1]));
      SB();
    }
    TS();
    wait1(lds, C_MP + (w ^ 1) * 4, 13);
#pragma unroll
    for (int q = 0; q < 4; ++q) xch[(w * 4 + q) * 64 + lane] = f32x4{acc[1][4 * q], acc[1][4 * q + 1], acc[1][4 * q + 2], acc[1][4 * q + 3]};
    post(lds, mymp, 14);
    wait1(lds, C_MP + (w ^ 2) * 4, 14);
    float s1 = 0.f, s2 = 0.f;
#pragma unroll
    for (int q = 0; q < 4; ++q) {
      const f32x4 t = xch[((w ^ 2) * 4 + q) * 64 + lane];
      const unsigned ua = pk2v(pkadd(P2(acc[0], 2 * q), P2(t, 0)));
      const unsigned ub = pk2v(pkadd(P2(acc[0], 2 * q + 1), P2(t, 1)));
      hres[q >> 1][(q & 1) * 2] = ua;
      hres[q >> 1][(q & 1) * 2 + 1] = ub;
    }
    const int so = WS_H + (m32 * 32 + 4 * mem + 2 * kh) * 1024;
    post(lds, mymp, 15);
    hres[0] = clean4(hres[0]); hres[1] = clean4(hres[1]);
    SB(); WAITV(0);
    ST128(hres[0], rw, lane * 16, so);
    ST128(hres[1], rw, lane * 16, so + 1024);
    SB();
#pragma unroll
    for (int q = 0; q < 8; ++q) {
      const unsigned u = hres[q >> 2][q & 3];
      const float e0 = bflo(u), e1 = bfhi(u);
      s1 += e0 + e1;
      s2 += e0 * e0 + e1 * e1;
    }
    s1 = half_sum(s1); s2 = half_sum(s2);
    const v2u st = {clean(asu(s1)), clean(asu(s2))};
    ST64(st, rw, lane < 32 ? lane * 8 : OOB, WS_HS + m32 * 4096 + (mem * 2 + kh) * 256);
    TS();
  }

  constexpr int NG = NG_;
  v4u af[NG][3];
  const int aso = WS_ACT + (m32 * 86 + 43 * kh) * 1024;
  auto ldA = [&](int g) LAM {
#pragma unroll
    for (int i = 0; i < 3; ++i) if (3 * g + i < 43) af[g % NG][i] = LD128(rw, lane * 16, aso + (3 * g + i) * 1024);
  };
  auto okA = [&](int g) LAM {
    unsigned m = max4(af[g % NG][0]);
    if (g < 14) m = umax(m, umax(max4(af[g % NG][1]), max4(af[g % NG][2])));
    return all_lanes(m != POISON);
  };
  {
    const LB tc = mkbase(ldsb + (4 * h2 + q4) * 192 + trcol);
    const int c32 = pp * 4 + w;
    v4u R[2][6];
    wait4(lds, C_MP, 15);
    wait4(lds, C_LAND, LB0 + 10);
#pragma unroll
    for (int s = 0; s < 6; ++s) R[0][s] = tr_frag(tc, slot_of(6) + (s % 3) * 64 + (s / 3) * 3072, 1536);
    v4u hf[NH_][4];
    const int hso = WS_H + c32 * 32 * 1024;
    auto ldH = [&](int g) LAM {
#pragma unroll
      for (int i = 0; i < 4; ++i) hf[g % NH_][i] = LD128(rw, lane * 16, hso + (4 * g + i) * 1024);
    };
    auto okH = [&](int g) LAM {
      return all_lanes(umax(umax(max4(hf[g % NH_][0]), max4(hf[g % NH_][1])), umax(max4(hf[g % NH_][2]), max4(hf[g % NH_][3]))) != POISON);
    };
    __builtin_amdgcn_s_sleep(FIRSTSLEEP);
#pragma unroll
    for (int g = 0; g < NH_; ++g) { ldH(g); SB(); }
    SB();
    f32x16 acc[3] = {};
    f32x2 st;
    f32x4 cs[8], bb[8];
    v4u pk4;
    unsigned cbase = ldsb + L_CS1 + 16 * h2;
    asm volatile("" : "+v"(cbase));
#pragma unroll
    for (int j = 0; j < 16; ++j) {
      if ((j & 1) == 0) {
        const int g = j >> 1;
        if (UNLIKELY(!okH(g))) {
#pragma nounroll
          for (unsigned sp = 0; sp < SPIN_CAP; ++sp) {
            MEMB();
            __builtin_amdgcn_s_sleep(PSLEEP);
            ldH(g);
#pragma unroll
            for (int d = 1; d < NH_; ++d) if (g + d < 8 && !okH(g + d)) ldH(g + d);
            __builtin_amdgcn_s_waitcnt(0x0F70);
            if (okH(g)) break;
          }
        }
        if (j == 6 && UNLIKELY(min4(pk4) < (unsigned)(LB0 + 14))) wait4(lds, C_LAND, LB0 + 14);
        if (j == CHOIST && UNLIKELY(min4(pk4) < 3u)) wait4(lds, C_HP, 3);
        MEMB(); SB();
        if (j == 0) { post(lds, mymp, 20); TS(); }
        if (LATE24 && j == LATE24) post(lds, mymp, 30);
      }
#pragma unroll
      for (int s = 0; s < 6; ++s) {
        const int ks = 2 * j + s / 3, tt = s % 3;
        if (j + 1 < 16) R[(j + 1) & 1][s] = tr_frag(tc, slot_of(6 + ((j + 1) >> 1)) + tt * 64 + (((j + 1) & 1) * 2 + s / 3) * 3072, 1536);
        acc[tt] = MFMA32(R[j & 1][s], hf[(ks >> 2) % NH_][ks & 3], acc[tt]);
        if (tt == 2 && ks + 4 * NH_ < 32) hf[(ks >> 2) % NH_][ks & 3] = LD128(rw, lane * 16, hso + (ks + 4 * NH_) * 1024);
        if (s == 0 && j == 5) pk4 = *(volatile LAS v4u*)(lds + C_LAND);
        if (s == 0 && j == CHOIST - 1) pk4 = *(volatile LAS v4u*)(lds + C_HP);
        if (j >= CHOIST && j < CHOIST + 3) {
          const int t = (j - CHOIST) * 6 + s;
          const int col = ((t >> 1) < 6 ? (t >> 2) * 32 : 80) + 8 * ((t >> 1) & 1);
          if (t < 24 - 4 * HT) { if (t & 1) bb[t >> 1] = *(const LAS f32x4*)(size_t)(cbase + 768 + col * 4); else cs[t >> 1] = *(const LAS f32x4*)(size_t)(cbase + col * 4); }
          if (t == 24 - 4 * HT) st = *(const LAS f32x2*)(lds + L_ST2 + (w * 32 + r32) * 8);
        }
        SB();
      }
    }
    TS();
#pragma unroll
    for (int t = 0; t < HT; ++t) {
      xch[(w * 6 + 2 * t) * 64 + lane] = f32x4{acc[t][8], acc[t][9], acc[t][10], acc[t][11]};
      xch[(w * 6 + 2 * t + 1) * 64 + lane] = f32x4{acc[t][12], acc[t][13], acc[t][14], acc[t][15]};
    }
    post(lds, mymp, 40);
    {
#pragma unroll
      for (int u = 0; u < 6 - HT; ++u) {
        const int t = u < 3 ? u : 2, half = u < 3 ? 0 : 1;
        if (t < 2 || ntile == 3) {
          f32x2 o[4];
#pragma unroll
          for (int i = 0; i < 4; ++i) o[i] = pkfma(P2(cs[2 * u + (i >> 1)], i & 1), dup(st[0]), P2(bb[2 * u + (i >> 1)], i & 1));
#pragma unroll
          for (int i = 0; i < 4; ++i) o[i] = pkfma(P2(acc[t], 4 * half + i), dup(st[1]), o[i]);
          const v4u ao = mish8(o);
          ST128(clean4(ao), rw, lane * 16, WS_ACT + (c32 * 86 + posA(t, half)) * 1024);
        }
        SB();
      }
    }
    TS();
  }

  {
    v4u R[2][6];
    unsigned landed = wait4(lds, C_LAND, LB0 + 16);
    wait4(lds, C_MP, 40);
#pragma unroll
    for (int s = 0; s < 3; ++s) { R[0][s] = tr_frag(ta0, slot_of(14) + s * 2048, 1024); R[0][3 + s] = tr_frag(ta1, slot_of(14) + s * 2048, 1024); }
    __builtin_amdgcn_s_sleep(DSLEEP);
#pragma unroll
    for (int g = 0; g < NG; ++g) { ldA(g); SB(); }
    SB();
    f32x16 acc[2] = {};
#pragma unroll
    for (int q = 0; q < 8; ++q) { acc[0][2 * q] = bflo(hres[q >> 2][q & 3]); acc[0][2 * q + 1] = bfhi(hres[q >> 2][q & 3]); }
    asm volatile("" : "+v"(acc[0]));
#pragma unroll
    for (int c = 0; c < 15; ++c) {
      if (c + 1 < 15 && UNLIKELY(landed < (unsigned)(LB0 + 14 + c + 2))) landed = wait4(lds, C_LAND, LB0 + 14 + c + 2);
      if (UNLIKELY(!okA(c))) {
#pragma nounroll
        for (unsigned sp = 0; sp < SPIN_CAP; ++sp) {
          MEMB();
          __builtin_amdgcn_s_sleep(PSLEEP);
          ldA(c);
#pragma unroll
          for (int d = 1; d < NG; ++d) if (c + d < 15 && !okA(c + d)) ldA(c + d);
          __builtin_amdgcn_s_waitcnt(0x0F70);
          if (okA(c)) break;
        }
      }
      SB();
      if (c == 0) TS();
#pragma unroll
      for (int s = 0; s < 3; ++s) {
        const int ks = c * 3 + s;
        if (ks + 3 < 43) {
          R[(c + 1) & 1][s] = tr_frag(ta0, slot_of(14 + c + 1) + s * 2048, 1024);
          R[(c + 1) & 1][3 + s] = tr_frag(ta1, slot_of(14 + c + 1) + s * 2048, 1024);
        }
        if (ks < 43) {
          const v4u a = af[c % NG][s];
          acc[0] = MFMA32(R[c & 1][s], a, acc[0]);
          acc[1] = MFMA32(R[c & 1][3 + s], a, acc[1]);
          if (ks + 3 * NG < 43) af[c % NG][s] = LD128(rw, lane * 16, aso + (ks + 3 * NG) * 1024);
        }
        SB();
      }
      if (c == 8) wait4(lds, C_HP, 4);
      if (c == 1) post(lds, mymp, 52);
      if (c == 3) post(lds, mymp, 54);
    }
    TS();
#pragma unroll
    for (int q = 0; q < 4; ++q) xch[(w * 4 + q) * 64 + lane] = f32x4{acc[1][4 * q], acc[1][4 * q + 1], acc[1][4 * q + 2], acc[1][4 * q + 3]};
    post(lds, mymp, 70);
    wait1(lds, C_MP + (w ^ 2) * 4, 70);
    int lz = tid; asm volatile("" : "+v"(lz));
    const rsrc_t rout = __builtin_amdgcn_make_buffer_rsrc((void*)p.out, 0, S_ * D_ * 2, 0x00020000);
    const int so = (panel * 64 + mg * 32) * 1024 + (mem * 64 + kh * 32) * 2;
    const int oo = (lz & 31) * 1024 + ((lz >> 5) & 1) * 16 + so;
#pragma unroll
    for (int q2 = 0; q2 < 2; ++q2) {
      unsigned d[2][2];
#pragma unroll
      for (int qq = 0; qq < 2; ++qq) {
        const int q = 2 * q2 + qq;
        const f32x4 t = xch[((w ^ 2) * 4 + q) * 64 + lane];
        d[qq][0] = pk2v(pkadd(P2(acc[0], 2 * q), P2(t, 0)));
        d[qq][1] = pk2v(pkadd(P2(acc[0], 2 * q + 1), P2(t, 1)));
      }
      auto r0 = __builtin_amdgcn_permlane32_swap(d[0][0], d[1][0], false, false);
      auto r1 = __builtin_amdgcn_permlane32_swap(d[0][1], d[1][1], false, false);
      const unsigned x0 = r0[0], y0 = r0[1], x1 = r1[0], y1 = r1[1];
      __builtin_amdgcn_raw_buffer_store_b128(v4u{x0, x1, y0, y1}, rout, oo + q2 * 32, 0, 0);
    }
    TS();
    TSDUMP();
  }
}

extern "C" void kernel_launch(void* const* d_in, const int* in_sizes, int n_in, void* d_out, int out_size, void* d_ws, size_t ws_size,
                              hipStream_t stream) {
  Params p;
  p.x = (const char*)d_in[0]; p.wqkv = (const char*)d_in[1]; p.wo = (const char*)d_in[2];
  p.w1 = (const char*)d_in[4]; p.b1 = (const char*)d_in[5]; p.w2 = (const char*)d_in[6];
  p.out = (char*)d_out;
  const unsigned long long base = ((unsigned long long)(size_t)d_ws + 255ull) & ~255ull;
  unsigned long long nreg = NREG;
  while (nreg > 1 && nreg * (unsigned long long)RSTRIDE + 256ull > ws_size) nreg >>= 1;
  p.wsr = base | (nreg - 1);
  (void)hipFuncSetAttribute((const void*)fused_block, hipFuncAttributeMaxDynamicSharedMemorySize, LDS_BYTES);
  fused_block<<<256, 512, LDS_BYTES, stream>>>(p);
}
```
